```python
import torch

def _softmax(x, axis=-1):
    return torch.softmax(x, dim=axis)
def _gelu(x):
    return torch.nn.functional.gelu(x, approximate="tanh")
def _silu(x):
    return torch.nn.functional.silu(x)
def _sigmoid(x):
    return torch.sigmoid(x)
def _layernorm(x, axis=-1, eps=1e-5):
    m = x.mean(dim=axis, keepdim=True)
    v = x.var(dim=axis, keepdim=True, unbiased=False)
    return (x - m) / torch.sqrt(v + eps)
def _scatter_add(tgt, idx, src, axis=0):
    return tgt.clone().index_add_(axis, idx, src)
def _segment_sum(src, seg, n):
    out = torch.zeros((n,) + src.shape[1:], dtype=src.dtype)
    return out.index_add_(0, seg, src)
def _pad(x, pw):
    flat = tuple(v for pair in reversed(pw) for v in pair)
    return torch.nn.functional.pad(x, flat)

kernel_name = 'gated_mix__compressed_kv_cla__glu_gated_reglu__decode'

def setup_inputs():

    _w = (lambda a: (lambda t: t.to(torch.bfloat16) if t.is_floating_point() else t)(torch.as_tensor(a)))
    def _A_setup():
        B, H, Sq, Sk, D, M = 4, 32, 1, 4096, 128, 512
        Dc = 4 * D
        return {
            "x": _w((lambda *s: torch.randn(*s, dtype=torch.bfloat16) * 0.05)(B, Sq, M)),
            "c_kv": _w((lambda *s: torch.randn(*s, dtype=torch.bfloat16) * 0.4)(B, Sk, Dc)),
            "w_q": _w((lambda *s: torch.randn(*s, dtype=torch.bfloat16) * 6.4)(M, H, D) * (M ** -0.5)),
            "w_uk": _w((lambda *s: torch.randn(*s, dtype=torch.bfloat16) * 6.4)(Dc, H, D) * (Dc ** -0.5)),
            "w_uv": _w((lambda *s: torch.randn(*s, dtype=torch.bfloat16) * 0.4)(Dc, H, D) * (Dc ** -0.5)),
            "w_o": _w((lambda *s: torch.randn(*s, dtype=torch.bfloat16) * 0.4)(H, D, M) * ((H * D) ** -0.5)),
        }
    _A_in = _A_setup()

    def _B_setup():
        B, S, D, F = 4, 1, 512, 1792
        return {
            "x": _w((lambda *s: torch.randn(*s, dtype=torch.bfloat16) * 0.05)(B, S, D)),
            "W_gate": _w((lambda *s: torch.randn(*s, dtype=torch.bfloat16) * 0.8)(D, F) * (2.0 / D) ** 0.5),
            "W_up": _w((lambda *s: torch.randn(*s, dtype=torch.bfloat16) * 0.8)(D, F) * (2.0 / D) ** 0.5),
            "W_down": _w((lambda *s: torch.randn(*s, dtype=torch.bfloat16) * 0.8)(F, D) * (1.0 / F) ** 0.5),
        }
    _B_in = _B_setup()

    return {
        "A_x": _A_in["x"],
        "A_c_kv": _A_in["c_kv"],
        "A_w_q": _A_in["w_q"],
        "A_w_uk": _A_in["w_uk"],
        "A_w_uv": _A_in["w_uv"],
        "A_w_o": _A_in["w_o"],
        "B_x": _B_in["x"],
        "B_W_gate": _B_in["W_gate"],
        "B_W_up": _B_in["W_up"],
        "B_W_down": _B_in["W_down"],
        "gate_w": _w((lambda *s: torch.randn(*s, dtype=torch.bfloat16) * 0.05)(512) * 16.0),
    }

def reference(A_x, A_c_kv, A_w_q, A_w_uk, A_w_uv, A_w_o, B_x, B_W_gate, B_W_up, B_W_down, gate_w):
    def _A_ref(x, c_kv, w_q, w_uk, w_uv, w_o):
        B, Sq, M = x.shape[0], x.shape[1], x.shape[2]
        Sk = c_kv.shape[1]
        H, D = w_q.shape[1], w_q.shape[2]
        q = torch.einsum("bqm,mhd->bhqd", x, w_q)
        k = torch.einsum("bkc,chd->bhkd", c_kv, w_uk)
        v = torch.einsum("bkc,chd->bhkd", c_kv, w_uv)
        scores = torch.einsum("bhqd,bhkd->bhqk", q, k) * (D ** -0.5)
        off = Sk - Sq
        qi = torch.arange(Sq)[:, None]
        kj = torch.arange(Sk)[None, :]
        cmask = kj <= (qi + off)
        scores = torch.where(cmask[None, None], scores, -1e9)
        p = _softmax(scores, axis=-1)
        ctx = torch.einsum("bhqk,bhkd->bhqd", p, v)
        return torch.einsum("bhqd,hdm->bqm", ctx, w_o)

    def _B_ref(x, W_gate, W_up, W_down):
        gate = torch.einsum("bsd,df->bsf", x, W_gate)
        up = torch.einsum("bsd,df->bsf", x, W_up)
        gate = gate * (lambda x, dt: x.to(dt))(gate > 0, gate.dtype)
        h = gate * up
        return torch.einsum("bsf,fd->bsd", h, W_down)

    x = A_x
    g = _sigmoid((x * gate_w).sum(-1, keepdims=True))
    a = _A_ref(x, A_c_kv, A_w_q, A_w_uk, A_w_uv, A_w_o)
    b = _B_ref(x, B_W_gate, B_W_up, B_W_down)
    return g * a + (1 - g) * b
```

```cpp
#include <hip/hip_runtime.h>
#include <hip/hip_bf16.h>
#include <stdint.h>


typedef __attribute__((ext_vector_type(8))) short bf16x8;
typedef __attribute__((ext_vector_type(4))) float f32x4;
typedef __attribute__((ext_vector_type(16))) float f32x16;
typedef __attribute__((ext_vector_type(4))) unsigned u32x4;
typedef __attribute__((ext_vector_type(2))) unsigned u32x2;
typedef unsigned short u16;

#define GAS __attribute__((address_space(1)))
typedef GAS unsigned gu32;
typedef GAS unsigned long long gu64;
#define RLX_AGENT __ATOMIC_RELAXED, __HIP_MEMORY_SCOPE_AGENT

#define WS_FLAGS   0
#define WS_GRAN    4096
#define WS_ZERO_BYTES 69632
#define WS_QABS    131072
#define WS_HBUF    262144
#define WS_STATS   294912
#define WS_PART    1048576

__device__ __forceinline__ u16 f2bf(float f) { return __builtin_bit_cast(u16, (__bf16)f); }
__device__ __forceinline__ float bf2f(u16 v) { return __builtin_bit_cast(float, (unsigned)v << 16); }
__device__ __forceinline__ float bfr(float f) { return bf2f(f2bf(f)); }
__device__ __forceinline__ unsigned pack2(float lo, float hi) { return (unsigned)f2bf(lo) | ((unsigned)f2bf(hi) << 16); }
__device__ __forceinline__ float lo_f(unsigned d) { return __builtin_bit_cast(float, d << 16); }
__device__ __forceinline__ float hi_f(unsigned d) { return __builtin_bit_cast(float, d & 0xffff0000u); }

__device__ __forceinline__ unsigned fsw(unsigned r) { return ((r & 3u) << 2) | ((0x78u >> (2u * ((r >> 2) & 3u))) & 3u); }

__device__ __forceinline__ bf16x8 as_frag(u32x4 v) { return __builtin_bit_cast(bf16x8, v); }

__device__ __forceinline__ bf16x8 colfrag(const u32x4 (&r)[8], int i) {
  const int u = i >> 1;
  const unsigned sel = (i & 1) ? 0x07060302u : 0x05040100u;
  u32x4 o;
  o[0] = __builtin_amdgcn_perm(r[1][u], r[0][u], sel);
  o[1] = __builtin_amdgcn_perm(r[3][u], r[2][u], sel);
  o[2] = __builtin_amdgcn_perm(r[5][u], r[4][u], sel);
  o[3] = __builtin_amdgcn_perm(r[7][u], r[6][u], sel);
  return as_frag(o);
}

#define K1_FFN_BLOCKS 28
__global__ __launch_bounds__(512) void k1_kernel(const u16* __restrict__ x, const u16* __restrict__ wq,
                                                 const u16* __restrict__ wuk, const u16* __restrict__ wg,
                                                 const u16* __restrict__ wu, unsigned char* __restrict__ ws) {
  __shared__ __attribute__((aligned(16))) float red[8][4][128];
  __shared__ __attribute__((aligned(16))) u16 qs[4][128];
  const int tid = threadIdx.x, w = tid >> 6, l = tid & 63, g = l >> 4, ch = l & 15;
  const int bid = blockIdx.x;
  const bool isF = bid < K1_FFN_BLOCKS;
  const int qi = bid - K1_FFN_BLOCKS;
  const int h = (qi & 7) + 8 * ((qi >> 3) & 3);
  const int c0 = (qi >> 5) * 64;
  const int f0 = bid * 64;

  const u16* base; int stride;
  if (isF) { base = (ch < 8 ? wg + f0 + ch * 8 : wu + f0 + (ch - 8) * 8); stride = 1792; }
  else     { base = wq + h * 128 + ch * 8; stride = 4096; }

  u32x4 r[2][8];
#pragma unroll
  for (int kb = 0; kb < 2; ++kb)
#pragma unroll
    for (int j = 0; j < 8; ++j)
      r[kb][j] = *(const u32x4*)(base + (size_t)(64 * w + 32 * kb + 8 * g + j) * stride);
  u32x4 xa[2];
#pragma unroll
  for (int kb = 0; kb < 2; ++kb) {
    xa[kb] = (u32x4){0u, 0u, 0u, 0u};
    if (ch < 4) xa[kb] = *(const u32x4*)(x + ch * 512 + 64 * w + 32 * kb + 8 * g);
  }
  u32x4 uk[4];
  if (!isF && w < 4) {
#pragma unroll
    for (int s = 0; s < 4; ++s)
      uk[s] = *(const u32x4*)(wuk + (size_t)(c0 + 16 * w + ch) * 4096 + h * 128 + 32 * s + 8 * g);
  }
  if (!isF && tid < 17) *(u32x4*)(ws + (size_t)(qi * 17 + tid) * 16) = (u32x4){0u, 0u, 0u, 0u};

  f32x4 acc[8];
#pragma unroll
  for (int i = 0; i < 8; ++i) acc[i] = (f32x4){0.f, 0.f, 0.f, 0.f};
#pragma unroll
  for (int kb = 0; kb < 2; ++kb)
#pragma unroll
    for (int i = 0; i < 8; ++i)
      acc[i] = __builtin_amdgcn_mfma_f32_16x16x32_bf16(as_frag(xa[kb]), colfrag(r[kb], i), acc[i], 0, 0, 0);

  if (l < 16) {
#pragma unroll
    for (int b = 0; b < 4; ++b) {
      *(f32x4*)&red[w][b][8 * ch]     = (f32x4){acc[0][b], acc[1][b], acc[2][b], acc[3][b]};
      *(f32x4*)&red[w][b][8 * ch + 4] = (f32x4){acc[4][b], acc[5][b], acc[6][b], acc[7][b]};
    }
  }
  __syncthreads();
  const int b = tid >> 7, d = tid & 127;
  if (isF) {
    if (d < 64) {
      float gs = 0.f, us = 0.f;
#pragma unroll
      for (int ww = 0; ww < 8; ++ww) { gs += red[ww][b][d]; us += red[ww][b][d + 64]; }
      gs = bfr(gs); us = bfr(us);
      const float rg = gs > 0.f ? gs : 0.f;
      ((u16*)(ws + WS_HBUF))[b * 1792 + f0 + d] = f2bf(rg * us);
    }
    return;
  }
  {
    float s = 0.f;
#pragma unroll
    for (int ww = 0; ww < 8; ++ww) s += red[ww][b][d];
    qs[b][d] = f2bf(s);
  }
  __syncthreads();
  if (w < 4) {
    f32x4 a2 = (f32x4){0.f, 0.f, 0.f, 0.f};
#pragma unroll
    for (int s = 0; s < 4; ++s) {
      u32x4 bq = (u32x4){0u, 0u, 0u, 0u};
      if (ch < 4) bq = *(const u32x4*)&qs[ch][32 * s + 8 * g];
      a2 = __builtin_amdgcn_mfma_f32_16x16x32_bf16(as_frag(uk[s]), as_frag(bq), a2, 0, 0, 0);
    }
    if (ch < 4) {
      u32x2 o; o[0] = pack2(a2[0], a2[1]); o[1] = pack2(a2[2], a2[3]);
      *(u32x2*)((u16*)(ws + WS_QABS) + (size_t)(ch * 32 + h) * 512 + c0 + 16 * w + 4 * g) = o;
    }
  }
}

#define LDS_BYTES 163840
#define SPIN_CAP (1u << 16)
#define RAW_BARRIER() do { asm volatile("s_waitcnt lgkmcnt(0)" ::: "memory"); __builtin_amdgcn_s_barrier(); asm volatile("" ::: "memory"); } while (0)
#define GLDS16(src, dst) __builtin_amdgcn_global_load_lds((const unsigned*)(src), (__attribute__((address_space(3))) unsigned*)(dst), 16, 0, 0)

__device__ __forceinline__ u32x2 tr_read(unsigned lds_addr) {
  u32x2 r; asm volatile("ds_read_b64_tr_b16 %0, %1" : "=v"(r) : "v"(lds_addr) : "memory"); return r;
}

__device__ __forceinline__ void attention_role(const u16* __restrict__ ckv, unsigned char* __restrict__ ws,
                                               unsigned char* smem, int u) {
  const int tid = threadIdx.x, l = tid & 63, g = l >> 4, ch = l & 15;
  const int w = __builtin_amdgcn_readfirstlane(tid >> 6);
  const int b = u >> 5, chunk = u & 31;
  unsigned char* sC = smem;
  unsigned char* sQ = smem + 131072;
  const u16* qabs = (const u16*)(ws + WS_QABS);

#pragma unroll
  for (int i = 0; i < 4; ++i) {
    const int hd = 4 * w + i;
    const unsigned c64 = (l & ~15u) | ((l & 15u) ^ fsw(hd & 15));
    GLDS16(qabs + (size_t)(b * 32 + hd) * 512 + c64 * 8, sQ + hd * 1024);
  }
  {
    const u16* crow = ckv + (size_t)(b * 4096 + chunk * 128) * 512;
#pragma unroll
    for (int p = 0; p < 4; ++p)
#pragma unroll
      for (int rg = 0; rg < 4; ++rg) {
        const int r = 16 * w + 4 * rg + g;
        const unsigned cc = (unsigned)ch ^ fsw(r & 15);
        GLDS16(crow + (size_t)r * 512 + p * 128 + cc * 8, sC + p * 32768 + (16 * w + 4 * rg) * 256);
      }
  }
  asm volatile("s_waitcnt vmcnt(16)" ::: "memory");
  RAW_BARRIER();
  u32x4 qf[2][16];
  const unsigned fs = fsw(ch);
#pragma unroll
  for (int t = 0; t < 2; ++t)
#pragma unroll
    for (int s = 0; s < 16; ++s) {
      const unsigned c64 = 4 * s + g;
      qf[t][s] = *(const u32x4*)(sQ + (16 * t + ch) * 1024 + ((c64 & ~15u) | ((c64 & 15u) ^ fs)) * 16);
    }
  RAW_BARRIER();
  f32x4 s0 = (f32x4){0.f, 0.f, 0.f, 0.f}, s1 = (f32x4){0.f, 0.f, 0.f, 0.f};
#pragma unroll
  for (int p = 0; p < 4; ++p) {
    if (p == 0) asm volatile("s_waitcnt vmcnt(12)" ::: "memory");
    else if (p == 1) asm volatile("s_waitcnt vmcnt(8)" ::: "memory");
    else if (p == 2) asm volatile("s_waitcnt vmcnt(4)" ::: "memory");
    else asm volatile("s_waitcnt vmcnt(0)" ::: "memory");
#pragma unroll
    for (int ss = 0; ss < 4; ++ss) {
      const u32x4 a = *(const u32x4*)(sC + p * 32768 + (16 * w + ch) * 256 + ((unsigned)(4 * ss + g) ^ fs) * 16);
      s0 = __builtin_amdgcn_mfma_f32_16x16x32_bf16(as_frag(a), as_frag(qf[0][4 * p + ss]), s0, 0, 0, 0);
      s1 = __builtin_amdgcn_mfma_f32_16x16x32_bf16(as_frag(a), as_frag(qf[1][4 * p + ss]), s1, 0, 0, 0);
    }
  }
  unsigned char* sP = sQ;
  float* wmax = (float*)(sQ + 8192);
  float* wsum = (float*)(sQ + 9216);
  {
    float m0 = fmaxf(fmaxf(s0[0], s0[1]), fmaxf(s0[2], s0[3]));
    float m1 = fmaxf(fmaxf(s1[0], s1[1]), fmaxf(s1[2], s1[3]));
    m0 = fmaxf(m0, __shfl_xor(m0, 16)); m1 = fmaxf(m1, __shfl_xor(m1, 16));
    m0 = fmaxf(m0, __shfl_xor(m0, 32)); m1 = fmaxf(m1, __shfl_xor(m1, 32));
    if (g == 0) { wmax[w * 32 + ch] = m0; wmax[w * 32 + 16 + ch] = m1; }
  }
  __syncthreads();
  const float cs = 0.08838834764831845f * 1.4426950408889634f;
  {
    float M0 = wmax[ch], M1 = wmax[16 + ch];
#pragma unroll
    for (int ww = 1; ww < 8; ++ww) { M0 = fmaxf(M0, wmax[ww * 32 + ch]); M1 = fmaxf(M1, wmax[ww * 32 + 16 + ch]); }
    u32x2 p0, p1;
    p0[0] = pack2(exp2f((s0[0] - M0) * cs), exp2f((s0[1] - M0) * cs));
    p0[1] = pack2(exp2f((s0[2] - M0) * cs), exp2f((s0[3] - M0) * cs));
    p1[0] = pack2(exp2f((s1[0] - M1) * cs), exp2f((s1[1] - M1) * cs));
    p1[1] = pack2(exp2f((s1[2] - M1) * cs), exp2f((s1[3] - M1) * cs));
    float l0 = (lo_f(p0[0]) + hi_f(p0[0])) + (lo_f(p0[1]) + hi_f(p0[1]));
    float l1 = (lo_f(p1[0]) + hi_f(p1[0])) + (lo_f(p1[1]) + hi_f(p1[1]));
    l0 += __shfl_xor(l0, 16); l1 += __shfl_xor(l1, 16);
    l0 += __shfl_xor(l0, 32); l1 += __shfl_xor(l1, 32);
    if (g == 0) { wsum[w * 32 + ch] = l0; wsum[w * 32 + 16 + ch] = l1; }
    const unsigned off = (((unsigned)(2 * w + (g >> 1)) ^ fs) * 16) + (g & 1) * 8;
    *(u32x2*)(sP + ch * 256 + off) = p0;
    *(u32x2*)(sP + (16 + ch) * 256 + off) = p1;
  }
  __syncthreads();
  const int hd32 = l & 31, hh = l >> 5;
  f32x16 o0, o1;
#pragma unroll
  for (int i = 0; i < 16; ++i) { o0[i] = 0.f; o1[i] = 0.f; }
  {
    const int pp = w >> 1;
    const unsigned blk = (l >> 4) & 1, q = (l & 15) >> 2, p4 = l & 3;
    const unsigned sCbase = (unsigned)(size_t)(sC) + pp * 32768;
    const unsigned fh = fsw(hd32 & 15);
#pragma unroll
    for (int ks = 0; ks < 8; ++ks) {
      const u32x4 pb = *(const u32x4*)(sP + hd32 * 256 + (((unsigned)(2 * ks + hh)) ^ fh) * 16);
      u32x2 a[2][2];
#pragma unroll
      for (int tt = 0; tt < 2; ++tt)
#pragma unroll
        for (int t2 = 0; t2 < 2; ++t2) {
          const unsigned row = 16 * ks + 8 * hh + 4 * t2 + q;
          const unsigned cidx = 2 * (w & 1) + tt;
          const unsigned cc = 4 * cidx + 2 * blk + (p4 >> 1);
          a[tt][t2] = tr_read(sCbase + row * 256 + ((cc ^ fsw(row & 15)) * 16) + 8 * (p4 & 1));
        }
      asm volatile("s_waitcnt lgkmcnt(0)" : "+v"(a[0][0]), "+v"(a[0][1]), "+v"(a[1][0]), "+v"(a[1][1]) :: "memory");
      __builtin_amdgcn_sched_barrier(0);
      u32x4 a0 = (u32x4){a[0][0][0], a[0][0][1], a[0][1][0], a[0][1][1]};
      u32x4 a1 = (u32x4){a[1][0][0], a[1][0][1], a[1][1][0], a[1][1][1]};
      o0 = __builtin_amdgcn_mfma_f32_32x32x16_bf16(as_frag(a0), as_frag(pb), o0, 0, 0, 0);
      o1 = __builtin_amdgcn_mfma_f32_32x32x16_bf16(as_frag(a1), as_frag(pb), o1, 0, 0, 0);
    }
  }
  __syncthreads();
  {
    const int cbase = 64 * w;
#pragma unroll
    for (int tt = 0; tt < 2; ++tt)
#pragma unroll
      for (int qq = 0; qq < 4; ++qq) {
        const unsigned c64 = (unsigned)((cbase + 32 * tt) >> 3) + qq;
        u32x2 v;
        if (tt == 0) { v[0] = pack2(o0[4 * qq], o0[4 * qq + 1]); v[1] = pack2(o0[4 * qq + 2], o0[4 * qq + 3]); }
        else         { v[0] = pack2(o1[4 * qq], o1[4 * qq + 1]); v[1] = pack2(o1[4 * qq + 2], o1[4 * qq + 3]); }
        *(u32x2*)(sC + hd32 * 1024 + ((c64 & ~15u) | ((c64 & 15u) ^ (unsigned)(hd32 & 15))) * 16 + 8 * hh) = v;
      }
  }
  __syncthreads();
  {
    const __amdgpu_buffer_rsrc_t prs = __builtin_amdgcn_make_buffer_rsrc(ws + WS_PART, 0, 128 * 32768, 0x00020000);
#pragma unroll
    for (int i = 0; i < 4; ++i) {
      const int hd = 4 * w + i;
      const u32x4 v = *(const u32x4*)(sC + hd * 1024 + ((l & ~15u) | ((l & 15u) ^ (unsigned)(hd & 15))) * 16);
      __builtin_amdgcn_raw_buffer_store_b128(v, prs, (unsigned)((u * 32 + hd) * 1024 + l * 16), 0, 16);
    }
    if (tid < 32) {
      float m = wmax[tid], s = wsum[tid];
#pragma unroll
      for (int ww = 1; ww < 8; ++ww) { m = fmaxf(m, wmax[ww * 32 + tid]); s += wsum[ww * 32 + tid]; }
      const unsigned long long v = (unsigned long long)__builtin_bit_cast(unsigned, m * cs) |
                                   ((unsigned long long)__builtin_bit_cast(unsigned, s) << 32);
      __hip_atomic_store((gu64*)(ws + WS_STATS) + (b * 32 + tid) * 32 + chunk, v, RLX_AGENT);
    }
  }
  asm volatile("s_waitcnt vmcnt(0)" ::: "memory");
  __syncthreads();
  if (tid == 0) __hip_atomic_store((gu32*)(ws + WS_FLAGS) + u, 1u, RLX_AGENT);
}

__device__ __forceinline__ void combine_role(const u16* __restrict__ x, const u16* __restrict__ wuv,
                                             const u16* __restrict__ wo, const u16* __restrict__ wd,
                                             const u16* __restrict__ gw, unsigned char* __restrict__ ws,
                                             u16* __restrict__ out, unsigned char* smem, int v) {
  const int tid = threadIdx.x, l = tid & 63, g = l >> 4, ch = l & 15;
  const int w = __builtin_amdgcn_readfirstlane(tid >> 6);
  const int h = (v & 7) + 8 * ((v >> 3) & 3), b = v >> 5;
  const bool isFinal = v < 64;
  const int col0 = ((v & 7) * 8 + ((v >> 3) & 7)) * 8;
  unsigned char* sW = smem;
  float* red = (float*)(smem + 131072);
  u16* cc = (u16*)(smem + 147456);
  float* red2 = (float*)(smem + 149504);
  float* redg = (float*)(smem + 153600);
  float* redo = (float*)(smem + 153856);

  const int bq = tid & 3, rg = tid >> 2;
  float accf[8];
  float gate = 0.f;
#pragma unroll
  for (int i = 0; i < 8; ++i) accf[i] = 0.f;
  if (isFinal) {
    const u16* hb = (const u16*)(ws + WS_HBUF) + bq * 1792 + 14 * rg;
    u32x4 wv[14]; u16 hv[14];
#pragma unroll
    for (int i = 0; i < 14; ++i) { wv[i] = *(const u32x4*)(wd + (size_t)(14 * rg + i) * 512 + col0); hv[i] = hb[i]; }
    float gp[4];
    const float gwv = bf2f(gw[tid]);
#pragma unroll
    for (int i = 0; i < 4; ++i) gp[i] = bfr(bf2f(x[i * 512 + tid]) * gwv);
#pragma unroll
    for (int i = 0; i < 14; ++i) {
      const float hf = bf2f(hv[i]);
#pragma unroll
      for (int j = 0; j < 4; ++j) { accf[2 * j] += hf * lo_f(wv[i][j]); accf[2 * j + 1] += hf * hi_f(wv[i][j]); }
    }
#pragma unroll
    for (int i = 0; i < 4; ++i) {
#pragma unroll
      for (int o = 1; o < 64; o <<= 1) gp[i] += __shfl_xor(gp[i], o);
    }
    if (l == 0) { redg[w * 4 + 0] = gp[0]; redg[w * 4 + 1] = gp[1]; redg[w * 4 + 2] = gp[2]; redg[w * 4 + 3] = gp[3]; }
  }
  __syncthreads();
  if (isFinal) {
    float s = 0.f;
#pragma unroll
    for (int ww = 0; ww < 8; ++ww) s += redg[ww * 4 + bq];
    s = bfr(s);
    gate = bfr(1.f / (1.f + __expf(-s)));
  }
#pragma unroll
  for (int kb = 0; kb < 2; ++kb)
#pragma unroll
    for (int j = 0; j < 8; ++j)
      GLDS16(wuv + (size_t)(64 * w + 32 * kb + 8 * g + j) * 4096 + h * 128 + ch * 8, sW + ((w * 2 + kb) * 8 + j) * 1024);
  u32x4 wov[32];
  if (isFinal) {
#pragma unroll
    for (int i = 0; i < 32; ++i) wov[i] = *(const u32x4*)(wo + (size_t)(32 * rg + i) * 512 + col0);
  }
  if (w == 0) {
    gu32* fl = (gu32*)(ws + WS_FLAGS) + b * 32 + (l & 31);
    for (unsigned spins = 0; spins < SPIN_CAP; ++spins) {
      const unsigned f = __hip_atomic_load(fl, RLX_AGENT);
      if (__all(f == 1u)) break;
      __builtin_amdgcn_s_sleep(1);
    }
  }
  asm volatile("s_waitcnt vmcnt(0)" ::: "memory");
  __syncthreads();
  {
    const __amdgpu_buffer_rsrc_t prs = __builtin_amdgcn_make_buffer_rsrc(ws + WS_PART, 0, 128 * 32768, 0x00020000);
    u32x4 pv[4];
#pragma unroll
    for (int i = 0; i < 4; ++i)
      pv[i] = __builtin_amdgcn_raw_buffer_load_b128(prs, (unsigned)(((b * 32 + 4 * w + i) * 32 + h) * 1024 + l * 16), 0, 16);
    const unsigned long long st = __hip_atomic_load((gu64*)(ws + WS_STATS) + (b * 32 + h) * 32 + (l & 31), RLX_AGENT);
    const float m = __builtin_bit_cast(float, (unsigned)st), ls = __builtin_bit_cast(float, (unsigned)(st >> 32));
    float M = m;
#pragma unroll
    for (int o = 1; o < 32; o <<= 1) M = fmaxf(M, __shfl_xor(M, o));
    const float wt = exp2f(m - M);
    float L = wt * ls;
#pragma unroll
    for (int o = 1; o < 32; o <<= 1) L += __shfl_xor(L, o);
    const float inv = 1.f / L;
    float a[8];
#pragma unroll
    for (int i = 0; i < 8; ++i) a[i] = 0.f;
#pragma unroll
    for (int i = 0; i < 4; ++i) {
      const float wi = __shfl(wt, 4 * w + i) * inv;
#pragma unroll
      for (int j = 0; j < 4; ++j) { a[2 * j] += wi * lo_f(pv[i][j]); a[2 * j + 1] += wi * hi_f(pv[i][j]); }
    }
    *(f32x4*)&red[w * 512 + l * 8] = (f32x4){a[0], a[1], a[2], a[3]};
    *(f32x4*)&red[w * 512 + l * 8 + 4] = (f32x4){a[4], a[5], a[6], a[7]};
  }
  __syncthreads();
  {
    float s = 0.f;
#pragma unroll
    for (int ww = 0; ww < 8; ++ww) s += red[ww * 512 + tid];
    const u16 hi = f2bf(s);
    cc[tid] = hi;
    cc[512 + tid] = f2bf(s - bf2f(hi));
  }
  __syncthreads();
  {
    f32x4 acc[8];
#pragma unroll
    for (int i = 0; i < 8; ++i) acc[i] = (f32x4){0.f, 0.f, 0.f, 0.f};
#pragma unroll
    for (int kb = 0; kb < 2; ++kb) {
      u32x4 xa = (u32x4){0u, 0u, 0u, 0u};
      if (ch < 2) xa = *(const u32x4*)&cc[ch * 512 + 64 * w + 32 * kb + 8 * g];
      u32x4 r[8];
#pragma unroll
      for (int j = 0; j < 8; ++j) r[j] = *(const u32x4*)(sW + ((w * 2 + kb) * 8 + j) * 1024 + l * 16);
#pragma unroll
      for (int i = 0; i < 8; ++i) acc[i] = __builtin_amdgcn_mfma_f32_16x16x32_bf16(as_frag(xa), colfrag(r, i), acc[i], 0, 0, 0);
    }
    if (l < 16) {
      *(f32x4*)&red2[w * 128 + 8 * ch]     = (f32x4){acc[0][0] + acc[0][1], acc[1][0] + acc[1][1], acc[2][0] + acc[2][1], acc[3][0] + acc[3][1]};
      *(f32x4*)&red2[w * 128 + 8 * ch + 4] = (f32x4){acc[4][0] + acc[4][1], acc[5][0] + acc[5][1], acc[6][0] + acc[6][1], acc[7][0] + acc[7][1]};
    }
  }
  __syncthreads();
  if (tid < 64) {
    float s0 = 0.f, s1 = 0.f;
#pragma unroll
    for (int ww = 0; ww < 8; ++ww) { s0 += red2[ww * 128 + 2 * tid]; s1 += red2[ww * 128 + 2 * tid + 1]; }
    const unsigned long long gv = (1ull << 32) | (unsigned long long)pack2(s0, s1);
    __hip_atomic_store((gu64*)(ws + WS_GRAN) + b * 2048 + h * 64 + tid, gv, RLX_AGENT);
  }
  if (!isFinal) return;
  {
    const __amdgpu_buffer_rsrc_t grs = __builtin_amdgcn_make_buffer_rsrc(ws + WS_GRAN, 0, 65536, 0x00020000);
    u32x4 gr[8];
    for (unsigned spins = 0; spins < SPIN_CAP; ++spins) {
      asm volatile("" ::: "memory");
      bool ok = true;
#pragma unroll
      for (int i = 0; i < 8; ++i) {
        gr[i] = __builtin_amdgcn_raw_buffer_load_b128(grs, (unsigned)((bq * 2048 + 16 * rg + 2 * i) * 8), 0, 16);
        ok = ok && (gr[i][1] == 1u) && (gr[i][3] == 1u);
      }
      if (__all(ok)) break;
      __builtin_amdgcn_s_sleep(1);
    }
    float acca[8];
#pragma unroll
    for (int i = 0; i < 8; ++i) acca[i] = 0.f;
#pragma unroll
    for (int i = 0; i < 8; ++i) {
      const float c4[4] = {lo_f(gr[i][0]), hi_f(gr[i][0]), lo_f(gr[i][2]), hi_f(gr[i][2])};
#pragma unroll
      for (int k = 0; k < 4; ++k) {
#pragma unroll
        for (int j = 0; j < 4; ++j) {
          acca[2 * j] += c4[k] * lo_f(wov[4 * i + k][j]);
          acca[2 * j + 1] += c4[k] * hi_f(wov[4 * i + k][j]);
        }
      }
    }
#pragma unroll
    for (int i = 0; i < 8; ++i) {
#pragma unroll
      for (int o = 4; o < 64; o <<= 1) { acca[i] += __shfl_xor(acca[i], o); accf[i] += __shfl_xor(accf[i], o); }
    }
    if (l < 4) {
      *(f32x4*)&redo[(w * 4 + l) * 16]      = (f32x4){acca[0], acca[1], acca[2], acca[3]};
      *(f32x4*)&redo[(w * 4 + l) * 16 + 4]  = (f32x4){acca[4], acca[5], acca[6], acca[7]};
      *(f32x4*)&redo[(w * 4 + l) * 16 + 8]  = (f32x4){accf[0], accf[1], accf[2], accf[3]};
      *(f32x4*)&redo[(w * 4 + l) * 16 + 12] = (f32x4){accf[4], accf[5], accf[6], accf[7]};
    }
    __syncthreads();
    if (tid < 32) {
      const int bb = tid & 3, cidx = tid >> 2;
      float a = 0.f, f = 0.f;
#pragma unroll
      for (int ww = 0; ww < 8; ++ww) { a += redo[(ww * 4 + bb) * 16 + cidx]; f += redo[(ww * 4 + bb) * 16 + 8 + cidx]; }
      a = bfr(a); f = bfr(f);
      const float r = bfr(gate * a) + bfr(bfr(1.f - gate) * f);
      out[bb * 512 + col0 + cidx] = f2bf(r);
    }
  }
}

__global__ __launch_bounds__(512, 2) void k2_kernel(const u16* __restrict__ x, const u16* __restrict__ ckv,
                                                    const u16* __restrict__ wuv, const u16* __restrict__ wo,
                                                    const u16* __restrict__ wd, const u16* __restrict__ gw,
                                                    unsigned char* __restrict__ ws, u16* __restrict__ out) {
  extern __shared__ __attribute__((aligned(16))) unsigned char smem[];
  const int bid = blockIdx.x;
  if (bid < 128) attention_role(ckv, ws, smem, bid);
  else combine_role(x, wuv, wo, wd, gw, ws, out, smem, bid - 128);
}

extern "C" void kernel_launch(void* const* d_in, const int* in_sizes, int n_in,
                              void* d_out, int out_size, void* d_ws, size_t ws_size,
                              hipStream_t stream) {
  const u16* x   = (const u16*)d_in[0];
  const u16* ckv = (const u16*)d_in[1];
  const u16* wq  = (const u16*)d_in[2];
  const u16* wuk = (const u16*)d_in[3];
  const u16* wuv = (const u16*)d_in[4];
  const u16* wo  = (const u16*)d_in[5];
  const u16* wg  = (const u16*)d_in[7];
  const u16* wu  = (const u16*)d_in[8];
  const u16* wd  = (const u16*)d_in[9];
  const u16* gw  = (const u16*)d_in[10];
  unsigned char* ws = (unsigned char*)d_ws;
  (void)hipFuncSetAttribute((const void*)k2_kernel, hipFuncAttributeMaxDynamicSharedMemorySize, LDS_BYTES);
  k1_kernel<<<256 + K1_FFN_BLOCKS, 512, 0, stream>>>(x, wq, wuk, wg, wu, ws);
  k2_kernel<<<256, 512, LDS_BYTES, stream>>>(x, ckv, wuv, wo, wd, gw, ws, (u16*)d_out);
}
```

```cpp
#include <hip/hip_runtime.h>
#include <hip/hip_bf16.h>
#include <stdint.h>

#define NT_CKV 2
#define NT_WUV 1
#define NT_WO 1
#define NT_WQ 1
#define NT_WUK 1
#define ZERO_AUX 16
#define NT_FFN 1

typedef __attribute__((ext_vector_type(8))) short bf16x8;
typedef __attribute__((ext_vector_type(4))) float f32x4;
typedef __attribute__((ext_vector_type(16))) float f32x16;
typedef __attribute__((ext_vector_type(4))) unsigned u32x4;
typedef __attribute__((ext_vector_type(2))) unsigned u32x2;
typedef unsigned short u16;

#define GAS __attribute__((address_space(1)))
typedef GAS unsigned gu32;
typedef GAS unsigned long long gu64;
#define RLX_AGENT __ATOMIC_RELAXED, __HIP_MEMORY_SCOPE_AGENT

#define WS_OPART   0
#define WS_FPART   262144
#define WS_STATS   720896
#define WS_PART    753664
#define WS_ZERO_BYTES (753664 + 4194304)
#define WS_QABS    5242880
#define WS_HBUF    5505024

#define STAMP(ws, slot) do {} while (0)

__device__ __forceinline__ u16 f2bf(float f) { return __builtin_bit_cast(u16, (__bf16)f); }
__device__ __forceinline__ float bf2f(u16 v) { return __builtin_bit_cast(float, (unsigned)v << 16); }
__device__ __forceinline__ float bfr(float f) { return bf2f(f2bf(f)); }
__device__ __forceinline__ unsigned pack2(float lo, float hi) { return (unsigned)f2bf(lo) | ((unsigned)f2bf(hi) << 16); }
__device__ __forceinline__ float lo_f(unsigned d) { return __builtin_bit_cast(float, d << 16); }
__device__ __forceinline__ float hi_f(unsigned d) { return __builtin_bit_cast(float, d & 0xffff0000u); }
__device__ __forceinline__ unsigned nz(unsigned d) { return d ? d : 0x8000u; }
__device__ __forceinline__ bool allnz(u32x4 v) { return v[0] && v[1] && v[2] && v[3]; }

template <int NT> __device__ __forceinline__ u32x4 ld16(const void* p) {
  if (NT) return __builtin_nontemporal_load((const u32x4*)p);
  return *(const u32x4*)p;
}

template <int CTRL> __device__ __forceinline__ float dpp(float x) {
  return __builtin_bit_cast(float, __builtin_amdgcn_mov_dpp(__builtin_bit_cast(int, x), CTRL, 0xf, 0xf, true));
}
#define XOR1 0xB1
#define XOR2 0x4E
#define ROR4 0x124
#define ROR8 0x128
__device__ __forceinline__ float x16_max(float x) {
  auto s = __builtin_amdgcn_permlane16_swap(__float_as_uint(x), __float_as_uint(x), false, false);
  return fmaxf(__uint_as_float(s[0]), __uint_as_float(s[1]));
}
__device__ __forceinline__ float x32_max(float x) {
  auto s = __builtin_amdgcn_permlane32_swap(__float_as_uint(x), __float_as_uint(x), false, false);
  return fmaxf(__uint_as_float(s[0]), __uint_as_float(s[1]));
}
__device__ __forceinline__ float x16_sum(float x) {
  auto s = __builtin_amdgcn_permlane16_swap(__float_as_uint(x), __float_as_uint(x), false, false);
  return __uint_as_float(s[0]) + __uint_as_float(s[1]);
}
__device__ __forceinline__ float x32_sum(float x) {
  auto s = __builtin_amdgcn_permlane32_swap(__float_as_uint(x), __float_as_uint(x), false, false);
  return __uint_as_float(s[0]) + __uint_as_float(s[1]);
}

__device__ __forceinline__ unsigned fsw(unsigned r) { return ((r & 3u) << 2) | ((0x78u >> (2u * ((r >> 2) & 3u))) & 3u); }

__device__ __forceinline__ bf16x8 as_frag(u32x4 v) { return __builtin_bit_cast(bf16x8, v); }

__device__ __forceinline__ bf16x8 colfrag(const u32x4 (&r)[8], int i) {
  const int u = i >> 1;
  const unsigned sel = (i & 1) ? 0x07060302u : 0x05040100u;
  u32x4 o;
  o[0] = __builtin_amdgcn_perm(r[1][u], r[0][u], sel);
  o[1] = __builtin_amdgcn_perm(r[3][u], r[2][u], sel);
  o[2] = __builtin_amdgcn_perm(r[5][u], r[4][u], sel);
  o[3] = __builtin_amdgcn_perm(r[7][u], r[6][u], sel);
  return as_frag(o);
}

#define K1_FFN_BLOCKS 56
#define K1_ZERO_BLOCKS 72
__global__ __launch_bounds__(512) void k1_kernel(const u16* __restrict__ x, const u16* __restrict__ wq,
                                                 const u16* __restrict__ wuk, const u16* __restrict__ wg,
                                                 const u16* __restrict__ wu, unsigned char* __restrict__ ws) {
  __shared__ __attribute__((aligned(16))) float red[16][4][128];
  __shared__ __attribute__((aligned(16))) u16 qs[4][128];
  const int tid = threadIdx.x, w = tid >> 6, l = tid & 63, g = l >> 4, ch = l & 15;
  const int bid = blockIdx.x;
  STAMP(ws, 0);
  if (bid >= 128 + K1_FFN_BLOCKS) {
    const __amdgpu_buffer_rsrc_t zrs = __builtin_amdgcn_make_buffer_rsrc(ws, 0, WS_ZERO_BYTES, 0x00020000);
    const int zi = bid - 128 - K1_FFN_BLOCKS;
#pragma unroll
    for (int i = 0; i < 9; ++i)
      __builtin_amdgcn_raw_buffer_store_b128((u32x4){0u, 0u, 0u, 0u}, zrs, (unsigned)(((i * K1_ZERO_BLOCKS + zi) * 512 + tid) * 16), 0, ZERO_AUX);
    return;
  }
  if (bid >= 128) {
    const int f0 = (bid - 128) * 32, half = ch >> 3;
    const u16* base = ((ch & 4) ? wu : wg) + f0 + 8 * (ch & 3);
    u32x4 r[8];
#pragma unroll
    for (int j = 0; j < 8; ++j) r[j] = ld16<NT_FFN>(base + (size_t)(256 * half + 32 * w + 8 * g + j) * 1792);
    u32x4 xa = (u32x4){0u, 0u, 0u, 0u};
    if (ch < 8) xa = *(const u32x4*)(x + (ch & 3) * 512 + 256 * (ch >> 2) + 32 * w + 8 * g);
    f32x4 acc[8];
#pragma unroll
    for (int i = 0; i < 8; ++i) {
      acc[i] = (f32x4){0.f, 0.f, 0.f, 0.f};
      acc[i] = __builtin_amdgcn_mfma_f32_16x16x32_bf16(as_frag(xa), colfrag(r, i), acc[i], 0, 0, 0);
    }
    STAMP(ws, 1);
    if (g == half) {
#pragma unroll
      for (int b = 0; b < 4; ++b) {
        *(f32x4*)&red[2 * w + half][b][8 * (ch & 7)]     = (f32x4){acc[0][b], acc[1][b], acc[2][b], acc[3][b]};
        *(f32x4*)&red[2 * w + half][b][8 * (ch & 7) + 4] = (f32x4){acc[4][b], acc[5][b], acc[6][b], acc[7][b]};
      }
    }
    __syncthreads();
    if (tid < 128) {
      const int b = tid >> 5, d = tid & 31;
      float gs = 0.f, us = 0.f;
#pragma unroll
      for (int ww = 0; ww < 16; ++ww) { gs += red[ww][b][d]; us += red[ww][b][d + 32]; }
      gs = bfr(gs); us = bfr(us);
      const float rg = gs > 0.f ? gs : 0.f;
      ((u16*)(ws + WS_HBUF))[b * 1792 + f0 + d] = f2bf(rg * us);
    }
    return;
  }
  const int qi = bid;
  const int h = (qi & 7) + 8 * ((qi >> 3) & 3);
  const int c0 = (qi >> 5) * 128;
  const u16* base = wq + h * 128 + ch * 8;
  u32x4 r[2][8];
#pragma unroll
  for (int kb = 0; kb < 2; ++kb)
#pragma unroll
    for (int j = 0; j < 8; ++j)
      r[kb][j] = ld16<NT_WQ>(base + (size_t)(64 * w + 32 * kb + 8 * g + j) * 4096);
  u32x4 xa[2];
#pragma unroll
  for (int kb = 0; kb < 2; ++kb) {
    xa[kb] = (u32x4){0u, 0u, 0u, 0u};
    if (ch < 4) xa[kb] = *(const u32x4*)(x + ch * 512 + 64 * w + 32 * kb + 8 * g);
  }
  u32x4 uk[4];
#pragma unroll
  for (int s = 0; s < 4; ++s)
    uk[s] = ld16<NT_WUK>(wuk + (size_t)(c0 + 16 * w + ch) * 4096 + h * 128 + 32 * s + 8 * g);
  f32x4 acc[8];
#pragma unroll
  for (int i = 0; i < 8; ++i) acc[i] = (f32x4){0.f, 0.f, 0.f, 0.f};
#pragma unroll
  for (int kb = 0; kb < 2; ++kb)
#pragma unroll
    for (int i = 0; i < 8; ++i)
      acc[i] = __builtin_amdgcn_mfma_f32_16x16x32_bf16(as_frag(xa[kb]), colfrag(r[kb], i), acc[i], 0, 0, 0);
  STAMP(ws, 1);
  if (l < 16) {
#pragma unroll
    for (int b = 0; b < 4; ++b) {
      *(f32x4*)&red[w][b][8 * ch]     = (f32x4){acc[0][b], acc[1][b], acc[2][b], acc[3][b]};
      *(f32x4*)&red[w][b][8 * ch + 4] = (f32x4){acc[4][b], acc[5][b], acc[6][b], acc[7][b]};
    }
  }
  __syncthreads();
  {
    const int b = tid >> 7, d = tid & 127;
    float s = 0.f;
#pragma unroll
    for (int ww = 0; ww < 8; ++ww) s += red[ww][b][d];
    qs[b][d] = f2bf(s);
  }
  __syncthreads();
  {
    f32x4 a2 = (f32x4){0.f, 0.f, 0.f, 0.f};
#pragma unroll
    for (int s = 0; s < 4; ++s) {
      u32x4 bq = (u32x4){0u, 0u, 0u, 0u};
      if (ch < 4) bq = *(const u32x4*)&qs[ch][32 * s + 8 * g];
      a2 = __builtin_amdgcn_mfma_f32_16x16x32_bf16(as_frag(uk[s]), as_frag(bq), a2, 0, 0, 0);
    }
    if (ch < 4) {
      u32x2 o; o[0] = pack2(a2[0], a2[1]); o[1] = pack2(a2[2], a2[3]);
      *(u32x2*)((u16*)(ws + WS_QABS) + (size_t)(ch * 32 + h) * 512 + c0 + 16 * w + 4 * g) = o;
    }
  }
  STAMP(ws, 2);
}

#define LDS_BYTES 163840
#define SPIN_CAP (1u << 16)
#define RAW_BARRIER() do { asm volatile("s_waitcnt lgkmcnt(0)" ::: "memory"); __builtin_amdgcn_s_barrier(); asm volatile("" ::: "memory"); } while (0)
#define GLDS16(src, dst, aux) __builtin_amdgcn_global_load_lds((const unsigned*)(src), (__attribute__((address_space(3))) unsigned*)(dst), 16, 0, (aux))
#define TR_READ(dst, addr, off) asm volatile("ds_read_b64_tr_b16 %0, %1 offset:%2" : "=v"(dst) : "v"(addr), "i"(off) : "memory")
#define B128_READ(dst, addr) asm volatile("ds_read_b128 %0, %1" : "=v"(dst) : "v"(addr) : "memory")

__device__ __forceinline__ void attention_role(const u16* __restrict__ ckv, unsigned char* __restrict__ ws,
                                               unsigned char* smem, int u) {
  const int tid = threadIdx.x, l = tid & 63, g = l >> 4, ch = l & 15;
  const int w = __builtin_amdgcn_readfirstlane(tid >> 6);
  const int b = u >> 5, chunk = u & 31;
  unsigned char* sC = smem;
  unsigned char* sQ = smem + 131072;
  const u16* qabs = (const u16*)(ws + WS_QABS);
  STAMP(ws, 0);
#pragma unroll
  for (int i = 0; i < 4; ++i) {
    const int hd = 4 * w + i;
    const unsigned c64 = (l & ~15u) | ((l & 15u) ^ fsw(hd & 15));
    GLDS16(qabs + (size_t)(b * 32 + hd) * 512 + c64 * 8, sQ + hd * 1024, 0);
  }
  {
    const u16* crow = ckv + (size_t)(b * 4096 + chunk * 128) * 512;
#pragma unroll
    for (int p = 0; p < 4; ++p)
#pragma unroll
      for (int rg = 0; rg < 4; ++rg) {
        const int r = 16 * w + 4 * rg + g;
        const unsigned cc = (unsigned)ch ^ fsw(r & 15);
        GLDS16(crow + (size_t)r * 512 + p * 128 + cc * 8, sC + p * 32768 + (16 * w + 4 * rg) * 256, NT_CKV);
      }
  }
  asm volatile("s_waitcnt vmcnt(16)" ::: "memory");
  RAW_BARRIER();
  STAMP(ws, 1);
  u32x4 qf[2][16];
  const unsigned fs = fsw(ch);
#pragma unroll
  for (int t = 0; t < 2; ++t)
#pragma unroll
    for (int s = 0; s < 16; ++s) {
      const unsigned c64 = 4 * s + g;
      qf[t][s] = *(const u32x4*)(sQ + (16 * t + ch) * 1024 + ((c64 & ~15u) | ((c64 & 15u) ^ fs)) * 16);
    }
  RAW_BARRIER();
  STAMP(ws, 2);
  f32x4 s0 = (f32x4){0.f, 0.f, 0.f, 0.f}, s1 = (f32x4){0.f, 0.f, 0.f, 0.f};
#pragma unroll
  for (int p = 0; p < 4; ++p) {
    if (p == 0) asm volatile("s_waitcnt vmcnt(12)" ::: "memory");
    else if (p == 1) asm volatile("s_waitcnt vmcnt(8)" ::: "memory");
    else if (p == 2) asm volatile("s_waitcnt vmcnt(4)" ::: "memory");
    else asm volatile("s_waitcnt vmcnt(0)" ::: "memory");
    u32x4 a[4];
#pragma unroll
    for (int ss = 0; ss < 4; ++ss)
      a[ss] = *(const u32x4*)(sC + p * 32768 + (16 * w + ch) * 256 + ((unsigned)(4 * ss + g) ^ fs) * 16);
#pragma unroll
    for (int ss = 0; ss < 4; ++ss) {
      s0 = __builtin_amdgcn_mfma_f32_16x16x32_bf16(as_frag(a[ss]), as_frag(qf[0][4 * p + ss]), s0, 0, 0, 0);
      s1 = __builtin_amdgcn_mfma_f32_16x16x32_bf16(as_frag(a[ss]), as_frag(qf[1][4 * p + ss]), s1, 0, 0, 0);
    }
  }
  STAMP(ws, 3);
  unsigned char* sP = sQ;
  float* wmax = (float*)(sQ + 8192);
  float* wsum = (float*)(sQ + 9216);
  {
    float m0 = fmaxf(fmaxf(s0[0], s0[1]), fmaxf(s0[2], s0[3]));
    float m1 = fmaxf(fmaxf(s1[0], s1[1]), fmaxf(s1[2], s1[3]));
    m0 = x32_max(x16_max(m0)); m1 = x32_max(x16_max(m1));
    if (g == 0) wmax[ch * 8 + w] = m0;
    if (g == 1) wmax[(16 + ch) * 8 + w] = m1;
  }
  RAW_BARRIER();
  const float cs = 0.08838834764831845f * 1.4426950408889634f;
  {
    const f32x4 a0 = *(const f32x4*)&wmax[ch * 8], a1 = *(const f32x4*)&wmax[ch * 8 + 4];
    const f32x4 b0 = *(const f32x4*)&wmax[(16 + ch) * 8], b1 = *(const f32x4*)&wmax[(16 + ch) * 8 + 4];
    const float M0 = fmaxf(fmaxf(fmaxf(a0[0], a0[1]), fmaxf(a0[2], a0[3])), fmaxf(fmaxf(a1[0], a1[1]), fmaxf(a1[2], a1[3])));
    const float M1 = fmaxf(fmaxf(fmaxf(b0[0], b0[1]), fmaxf(b0[2], b0[3])), fmaxf(fmaxf(b1[0], b1[1]), fmaxf(b1[2], b1[3])));
    u32x2 p0, p1;
    p0[0] = pack2(exp2f((s0[0] - M0) * cs), exp2f((s0[1] - M0) * cs));
    p0[1] = pack2(exp2f((s0[2] - M0) * cs), exp2f((s0[3] - M0) * cs));
    p1[0] = pack2(exp2f((s1[0] - M1) * cs), exp2f((s1[1] - M1) * cs));
    p1[1] = pack2(exp2f((s1[2] - M1) * cs), exp2f((s1[3] - M1) * cs));
    const unsigned off = (((unsigned)(2 * w + (g >> 1)) ^ fs) * 16) + (g & 1) * 8;
    *(u32x2*)(sP + ch * 256 + off) = p0;
    *(u32x2*)(sP + (16 + ch) * 256 + off) = p1;
    float l0 = (lo_f(p0[0]) + hi_f(p0[0])) + (lo_f(p0[1]) + hi_f(p0[1]));
    float l1 = (lo_f(p1[0]) + hi_f(p1[0])) + (lo_f(p1[1]) + hi_f(p1[1]));
    l0 = x32_sum(x16_sum(l0)); l1 = x32_sum(x16_sum(l1));
    if (g == 0) wsum[ch * 8 + w] = l0;
    if (g == 1) wsum[(16 + ch) * 8 + w] = l1;
  }
  RAW_BARRIER();
  STAMP(ws, 4);
  const int hd32 = l & 31, hh = l >> 5;
  f32x16 o0, o1;
#pragma unroll
  for (int i = 0; i < 16; ++i) { o0[i] = 0.f; o1[i] = 0.f; }
  {
    const unsigned blk = (l >> 4) & 1, q = (l & 15) >> 2, p4 = l & 3;
    const unsigned sCbase = (unsigned)(size_t)(sC) + (w >> 1) * 32768;
    const unsigned sPbase = (unsigned)(size_t)(sP) + hd32 * 256;
    const unsigned fh = fsw(hd32 & 15);
    unsigned ta[2][2];
#pragma unroll
    for (int tt = 0; tt < 2; ++tt)
#pragma unroll
      for (int t2 = 0; t2 < 2; ++t2) {
        const unsigned row = 8 * hh + 4 * t2 + q;
        const unsigned cc = 4 * (2 * (w & 1) + tt) + 2 * blk + (p4 >> 1);
        ta[tt][t2] = sCbase + row * 256 + ((cc ^ fsw(row)) * 16) + 8 * (p4 & 1);
      }
    u32x4 pb[8]; u32x2 a[8][2][2];
#define PV_ISSUE(ks) do { B128_READ(pb[ks], sPbase + (((unsigned)(2 * (ks) + hh)) ^ fh) * 16); \
      TR_READ(a[ks][0][0], ta[0][0], (ks) * 4096); TR_READ(a[ks][0][1], ta[0][1], (ks) * 4096); \
      TR_READ(a[ks][1][0], ta[1][0], (ks) * 4096); TR_READ(a[ks][1][1], ta[1][1], (ks) * 4096); } while (0)
#define PV_WAIT(ks, n) asm volatile("s_waitcnt lgkmcnt(" #n ")" : "+v"(pb[ks]), "+v"(a[ks][0][0]), "+v"(a[ks][0][1]), "+v"(a[ks][1][0]), "+v"(a[ks][1][1]) :: "memory")
#define PV_MMA(ks) do { \
      u32x4 a0 = (u32x4){a[ks][0][0][0], a[ks][0][0][1], a[ks][0][1][0], a[ks][0][1][1]}; \
      u32x4 a1 = (u32x4){a[ks][1][0][0], a[ks][1][0][1], a[ks][1][1][0], a[ks][1][1][1]}; \
      o0 = __builtin_amdgcn_mfma_f32_32x32x16_bf16(as_frag(a0), as_frag(pb[ks]), o0, 0, 0, 0); \
      o1 = __builtin_amdgcn_mfma_f32_32x32x16_bf16(as_frag(a1), as_frag(pb[ks]), o1, 0, 0, 0); } while (0)
    PV_ISSUE(0); PV_ISSUE(1); PV_ISSUE(2);
    PV_WAIT(0, 10); PV_MMA(0); __builtin_amdgcn_sched_barrier(0); PV_ISSUE(3);
    PV_WAIT(1, 10); PV_MMA(1); __builtin_amdgcn_sched_barrier(0); PV_ISSUE(4);
    PV_WAIT(2, 10); PV_MMA(2); __builtin_amdgcn_sched_barrier(0); PV_ISSUE(5);
    PV_WAIT(3, 10); PV_MMA(3); __builtin_amdgcn_sched_barrier(0); PV_ISSUE(6);
    PV_WAIT(4, 10); PV_MMA(4); __builtin_amdgcn_sched_barrier(0); PV_ISSUE(7);
    PV_WAIT(5, 10); PV_MMA(5);
    PV_WAIT(6, 5);  PV_MMA(6);
    PV_WAIT(7, 0);  PV_MMA(7);
  }
  RAW_BARRIER();
  STAMP(ws, 5);
  {
    unsigned char* st = sC + w * 4096;
#pragma unroll
    for (int tt = 0; tt < 2; ++tt)
#pragma unroll
      for (int qq = 0; qq < 4; ++qq) {
        u32x2 v;
        if (tt == 0) { v[0] = pack2(o0[4 * qq], o0[4 * qq + 1]); v[1] = pack2(o0[4 * qq + 2], o0[4 * qq + 3]); }
        else         { v[0] = pack2(o1[4 * qq], o1[4 * qq + 1]); v[1] = pack2(o1[4 * qq + 2], o1[4 * qq + 3]); }
        *(u32x2*)(st + hd32 * 128 + (((unsigned)(4 * tt + qq) ^ (unsigned)(hd32 & 7)) * 16) + 8 * hh) = v;
      }
    const __amdgpu_buffer_rsrc_t prs = __builtin_amdgcn_make_buffer_rsrc(ws + WS_PART, 0, 128 * 32768, 0x00020000);
#pragma unroll
    for (int n = 0; n < 4; ++n) {
      const int hd = 8 * n + (l >> 3);
      u32x4 v = *(const u32x4*)(st + hd * 128 + (((unsigned)(l & 7) ^ (unsigned)(hd & 7)) * 16));
      v[0] = nz(v[0]); v[1] = nz(v[1]); v[2] = nz(v[2]); v[3] = nz(v[3]);
      __builtin_amdgcn_raw_buffer_store_b128(v, prs, (unsigned)((u * 32 + hd) * 1024 + w * 128 + (l & 7) * 16), 0, 16);
    }
    if (tid < 32) {
      const f32x4 a0 = *(const f32x4*)&wmax[tid * 8], a1 = *(const f32x4*)&wmax[tid * 8 + 4];
      const f32x4 b0 = *(const f32x4*)&wsum[tid * 8], b1 = *(const f32x4*)&wsum[tid * 8 + 4];
      const float m = fmaxf(fmaxf(fmaxf(a0[0], a0[1]), fmaxf(a0[2], a0[3])), fmaxf(fmaxf(a1[0], a1[1]), fmaxf(a1[2], a1[3])));
      const float s = ((b0[0] + b0[1]) + (b0[2] + b0[3])) + ((b1[0] + b1[1]) + (b1[2] + b1[3]));
      const unsigned long long v = (unsigned long long)__builtin_bit_cast(unsigned, m * cs) |
                                   ((unsigned long long)__builtin_bit_cast(unsigned, s) << 32);
      __hip_atomic_store((gu64*)(ws + WS_STATS) + (b * 32 + tid) * 32 + chunk, v, RLX_AGENT);
    }
  }
  STAMP(ws, 6);
}

__device__ __forceinline__ unsigned nzf(float f) { const unsigned d = __builtin_bit_cast(unsigned, f); return d ? d : 0x80000000u; }

__device__ __forceinline__ void combine_role(const u16* __restrict__ x, const u16* __restrict__ wuv,
                                             const u16* __restrict__ wo, const u16* __restrict__ wd,
                                             const u16* __restrict__ gw, unsigned char* __restrict__ ws,
                                             u16* __restrict__ out, unsigned char* smem, int v) {
  const int tid = threadIdx.x, l = tid & 63, g = l >> 4, ch = l & 15;
  const int w = __builtin_amdgcn_readfirstlane(tid >> 6);
  const int h = (v & 7) + 8 * ((v >> 3) & 3), b = v >> 5;
  u16* cc = (u16*)(smem);
  float* red2 = (float*)(smem + 2048);
  u16* cx = (u16*)(smem + 6144);
  float* red3 = (float*)(smem + 8192);
  STAMP(ws, 0);
  if (v < 56 && w < 4) {
    u32x4 rd[8];
#pragma unroll
    for (int j = 0; j < 8; ++j) rd[j] = ld16<NT_FFN>(wd + (size_t)(32 * v + 8 * g + j) * 512 + 128 * w + 8 * ch);
    u32x4 ha = (u32x4){0u, 0u, 0u, 0u};
    if (ch < 4) ha = *(const u32x4*)((const u16*)(ws + WS_HBUF) + ch * 1792 + 32 * v + 8 * g);
    const __amdgpu_buffer_rsrc_t frs = __builtin_amdgcn_make_buffer_rsrc(ws + WS_FPART, 0, 56 * 8192, 0x00020000);
    f32x4 acc[8];
#pragma unroll
    for (int i = 0; i < 8; ++i) {
      acc[i] = (f32x4){0.f, 0.f, 0.f, 0.f};
      acc[i] = __builtin_amdgcn_mfma_f32_16x16x32_bf16(as_frag(ha), colfrag(rd, i), acc[i], 0, 0, 0);
    }
    if (l < 16) {
#pragma unroll
      for (int bb = 0; bb < 4; ++bb) {
        const unsigned off = (unsigned)(((v * 4 + bb) * 512 + 128 * w + 8 * ch) * 4);
        __builtin_amdgcn_raw_buffer_store_b128((u32x4){nzf(acc[0][bb]), nzf(acc[1][bb]), nzf(acc[2][bb]), nzf(acc[3][bb])}, frs, off, 0, 16);
        __builtin_amdgcn_raw_buffer_store_b128((u32x4){nzf(acc[4][bb]), nzf(acc[5][bb]), nzf(acc[6][bb]), nzf(acc[7][bb])}, frs, off + 16, 0, 16);
      }
    }
  }
  u32x4 r[2][8], ro[2][8];
#pragma unroll
  for (int kb = 0; kb < 2; ++kb)
#pragma unroll
    for (int j = 0; j < 8; ++j)
      r[kb][j] = ld16<NT_WUV>(wuv + (size_t)(64 * w + 32 * kb + 8 * g + j) * 4096 + h * 128 + ch * 8);
#pragma unroll
  for (int cg = 0; cg < 2; ++cg)
#pragma unroll
    for (int j = 0; j < 8; ++j)
      ro[cg][j] = ld16<NT_WO>(wo + (size_t)(h * 128 + 32 * (w >> 1) + 8 * g + j) * 512 + 128 * (2 * (w & 1) + cg) + ch * 8);
  float gate = 0.f, f[4] = {0.f, 0.f, 0.f, 0.f};
  if (v < 64 && w == 7) {
    const int rb = v >> 4, cs0 = (v & 15) * 32;
    const int i8 = l >> 3, c4 = l & 7;
    const u32x4 xv = *(const u32x4*)(x + rb * 512 + 8 * l), gv = *(const u32x4*)(gw + 8 * l);
    float gs = 0.f;
#pragma unroll
    for (int j = 0; j < 4; ++j) { gs += bfr(lo_f(xv[j]) * lo_f(gv[j])); gs += bfr(hi_f(xv[j]) * hi_f(gv[j])); }
    gs += dpp<XOR1>(gs); gs += dpp<XOR2>(gs); gs += dpp<ROR4>(gs); gs += dpp<ROR8>(gs);
    gs = x32_sum(x16_sum(gs));
    gate = bfr(1.f / (1.f + __expf(-bfr(gs))));
    const __amdgpu_buffer_rsrc_t frs = __builtin_amdgcn_make_buffer_rsrc(ws + WS_FPART, 0, 56 * 8192, 0x00020000);
    u32x4 fv[7];
    for (unsigned spins = 0; spins < SPIN_CAP; ++spins) {
      asm volatile("" ::: "memory");
      bool ok = true;
#pragma unroll
      for (int n = 0; n < 7; ++n) {
        fv[n] = __builtin_amdgcn_raw_buffer_load_b128(frs, (unsigned)((((8 * n + i8) * 4 + rb) * 512 + cs0 + 4 * c4) * 4), 0, 16);
        ok = ok && allnz(fv[n]);
      }
      if (__all(ok)) break;
    }
#pragma unroll
    for (int j = 0; j < 4; ++j) {
      f[j] = 0.f;
#pragma unroll
      for (int n = 0; n < 7; ++n) f[j] += __uint_as_float(fv[n][j]);
      f[j] += dpp<ROR8>(f[j]); f[j] = x32_sum(x16_sum(f[j]));
    }
  }
  STAMP(ws, 2);
  {
    gu64* sp = (gu64*)(ws + WS_STATS) + (b * 32 + h) * 32 + (l & 31);
    unsigned long long st = 0;
    for (unsigned spins = 0; spins < SPIN_CAP; ++spins) {
      st = __hip_atomic_load(sp, RLX_AGENT);
      if (__all((unsigned)(st >> 32) != 0u)) break;
      __builtin_amdgcn_s_sleep(1);
    }
    STAMP(ws, 3);
    const __amdgpu_buffer_rsrc_t prs = __builtin_amdgcn_make_buffer_rsrc(ws + WS_PART, 0, 128 * 32768, 0x00020000);
    u32x4 pv[4];
    for (unsigned spins = 0; spins < SPIN_CAP; ++spins) {
      asm volatile("" ::: "memory");
#pragma unroll
      for (int n = 0; n < 4; ++n)
        pv[n] = __builtin_amdgcn_raw_buffer_load_b128(prs, (unsigned)(((b * 32 + 8 * n + (l >> 3)) * 32 + h) * 1024 + w * 128 + (l & 7) * 16), 0, 16);
      if (__all(allnz(pv[0]) && allnz(pv[1]) && allnz(pv[2]) && allnz(pv[3]))) break;
    }
    const float m = __builtin_bit_cast(float, (unsigned)st), ls = __builtin_bit_cast(float, (unsigned)(st >> 32));
    float M = m;
    M = fmaxf(M, dpp<XOR1>(M)); M = fmaxf(M, dpp<XOR2>(M)); M = fmaxf(M, dpp<ROR4>(M)); M = fmaxf(M, dpp<ROR8>(M));
    M = x16_max(M);
    const float wt = exp2f(m - M);
    float L = wt * ls;
    L += dpp<XOR1>(L); L += dpp<XOR2>(L); L += dpp<ROR4>(L); L += dpp<ROR8>(L);
    L = x16_sum(L);
    const float inv = 1.f / L;
    float a[8];
#pragma unroll
    for (int i = 0; i < 8; ++i) a[i] = 0.f;
#pragma unroll
    for (int n = 0; n < 4; ++n) {
      const float wi = __shfl(wt, 8 * n + (l >> 3));
#pragma unroll
      for (int j = 0; j < 4; ++j) { a[2 * j] += wi * lo_f(pv[n][j]); a[2 * j + 1] += wi * hi_f(pv[n][j]); }
    }
#pragma unroll
    for (int i = 0; i < 8; ++i) { a[i] += dpp<ROR8>(a[i]); a[i] = x32_sum(x16_sum(a[i])) * inv; }
    if (l < 16) {
      u32x4 o;
#pragma unroll
      for (int j = 0; j < 4; ++j) {
        const unsigned hi = pack2(a[2 * j], a[2 * j + 1]);
        o[j] = (l < 8) ? hi : pack2(a[2 * j] - lo_f(hi), a[2 * j + 1] - hi_f(hi));
      }
      *(u32x4*)&cc[(w * 2 + (l >> 3)) * 64 + 8 * (l & 7)] = o;
    }
  }
  STAMP(ws, 4);
  {
    f32x4 acc[8];
#pragma unroll
    for (int i = 0; i < 8; ++i) acc[i] = (f32x4){0.f, 0.f, 0.f, 0.f};
#pragma unroll
    for (int kb = 0; kb < 2; ++kb) {
      u32x4 xa = (u32x4){0u, 0u, 0u, 0u};
      if (ch < 2) xa = *(const u32x4*)&cc[(w * 2 + ch) * 64 + 32 * kb + 8 * g];
#pragma unroll
      for (int i = 0; i < 8; ++i) acc[i] = __builtin_amdgcn_mfma_f32_16x16x32_bf16(as_frag(xa), colfrag(r[kb], i), acc[i], 0, 0, 0);
    }
    if (l < 16) {
      *(f32x4*)&red2[w * 128 + 8 * ch]     = (f32x4){acc[0][0] + acc[0][1], acc[1][0] + acc[1][1], acc[2][0] + acc[2][1], acc[3][0] + acc[3][1]};
      *(f32x4*)&red2[w * 128 + 8 * ch + 4] = (f32x4){acc[4][0] + acc[4][1], acc[5][0] + acc[5][1], acc[6][0] + acc[6][1], acc[7][0] + acc[7][1]};
    }
  }
  __syncthreads();
  u32x4 xa = (u32x4){0u, 0u, 0u, 0u};
  {
    float s[8];
#pragma unroll
    for (int j = 0; j < 8; ++j) s[j] = 0.f;
    const int d0 = 32 * (w >> 1) + 8 * g;
#pragma unroll
    for (int ww = 0; ww < 8; ++ww) {
      const f32x4 u0 = *(const f32x4*)&red2[ww * 128 + d0], u1 = *(const f32x4*)&red2[ww * 128 + d0 + 4];
      s[0] += u0[0]; s[1] += u0[1]; s[2] += u0[2]; s[3] += u0[3]; s[4] += u1[0]; s[5] += u1[1]; s[6] += u1[2]; s[7] += u1[3];
    }
    if (ch == 0) xa = (u32x4){pack2(s[0], s[1]), pack2(s[2], s[3]), pack2(s[4], s[5]), pack2(s[6], s[7])};
  }
  {
#pragma unroll
    for (int cg = 0; cg < 2; ++cg) {
      f32x4 acc[8];
#pragma unroll
      for (int i = 0; i < 8; ++i) {
        acc[i] = (f32x4){0.f, 0.f, 0.f, 0.f};
        acc[i] = __builtin_amdgcn_mfma_f32_16x16x32_bf16(as_frag(xa), colfrag(ro[cg], i), acc[i], 0, 0, 0);
      }
      if (l < 16) {
        float* d = &red3[(w >> 1) * 512 + 128 * (2 * (w & 1) + cg) + 8 * ch];
        *(f32x4*)d = (f32x4){acc[0][0], acc[1][0], acc[2][0], acc[3][0]};
        *(f32x4*)(d + 4) = (f32x4){acc[4][0], acc[5][0], acc[6][0], acc[7][0]};
      }
    }
  }
  __syncthreads();
  if (tid < 128) {
    const f32x4 u0 = *(const f32x4*)&red3[4 * tid], u1 = *(const f32x4*)&red3[512 + 4 * tid];
    const f32x4 u2 = *(const f32x4*)&red3[1024 + 4 * tid], u3 = *(const f32x4*)&red3[1536 + 4 * tid];
    const __amdgpu_buffer_rsrc_t ors = __builtin_amdgcn_make_buffer_rsrc(ws + WS_OPART, 0, 128 * 2048, 0x00020000);
    u32x4 o;
#pragma unroll
    for (int j = 0; j < 4; ++j) o[j] = nzf((u0[j] + u1[j]) + (u2[j] + u3[j]));
    __builtin_amdgcn_raw_buffer_store_b128(o, ors, (unsigned)(((b * 32 + h) * 512 + 4 * tid) * 4), 0, 16);
  }
  STAMP(ws, 5);
  if (v < 64 && w == 7) {
    const int rb = v >> 4, cs0 = (v & 15) * 32;
    const int i8 = l >> 3, c4 = l & 7;
    const __amdgpu_buffer_rsrc_t ors = __builtin_amdgcn_make_buffer_rsrc(ws + WS_OPART, 0, 128 * 2048, 0x00020000);
    u32x4 av[4];
    for (unsigned spins = 0; spins < SPIN_CAP; ++spins) {
      asm volatile("" ::: "memory");
#pragma unroll
      for (int n = 0; n < 4; ++n)
        av[n] = __builtin_amdgcn_raw_buffer_load_b128(ors, (unsigned)(((rb * 32 + 8 * n + i8) * 512 + cs0 + 4 * c4) * 4), 0, 16);
      if (__all(allnz(av[0]) && allnz(av[1]) && allnz(av[2]) && allnz(av[3]))) break;
    }
    float res[4];
#pragma unroll
    for (int j = 0; j < 4; ++j) {
      float a = (__uint_as_float(av[0][j]) + __uint_as_float(av[1][j])) +
                (__uint_as_float(av[2][j]) + __uint_as_float(av[3][j]));
      a += dpp<ROR8>(a); a = x32_sum(x16_sum(a));
      res[j] = bfr(gate * bfr(a)) + bfr(bfr(1.f - gate) * bfr(f[j]));
    }
    if (l < 8) *(u32x2*)(out + rb * 512 + cs0 + 4 * l) = (u32x2){pack2(res[0], res[1]), pack2(res[2], res[3])};
  }
}

__global__ __launch_bounds__(512, 2) void k2_kernel(const u16* __restrict__ x, const u16* __restrict__ ckv,
                                                    const u16* __restrict__ wuv, const u16* __restrict__ wo,
                                                    const u16* __restrict__ wd, const u16* __restrict__ gw,
                                                    unsigned char* __restrict__ ws, u16* __restrict__ out) {
  extern __shared__ __attribute__((aligned(16))) unsigned char smem[];
  const int bid = blockIdx.x;
  if (bid < 128) attention_role(ckv, ws, smem, bid);
  else combine_role(x, wuv, wo, wd, gw, ws, out, smem, bid - 128);
}

extern "C" void kernel_launch(void* const* d_in, const int* in_sizes, int n_in,
                              void* d_out, int out_size, void* d_ws, size_t ws_size,
                              hipStream_t stream) {
  const u16* x   = (const u16*)d_in[0];
  const u16* ckv = (const u16*)d_in[1];
  const u16* wq  = (const u16*)d_in[2];
  const u16* wuk = (const u16*)d_in[3];
  const u16* wuv = (const u16*)d_in[4];
  const u16* wo  = (const u16*)d_in[5];
  const u16* wg  = (const u16*)d_in[7];
  const u16* wu  = (const u16*)d_in[8];
  const u16* wd  = (const u16*)d_in[9];
  const u16* gw  = (const u16*)d_in[10];
  unsigned char* ws = (unsigned char*)d_ws;
  (void)hipFuncSetAttribute((const void*)k2_kernel, hipFuncAttributeMaxDynamicSharedMemorySize, LDS_BYTES);
  k1_kernel<<<128 + K1_FFN_BLOCKS + K1_ZERO_BLOCKS, 512, 0, stream>>>(x, wq, wuk, wg, wu, ws);
  k2_kernel<<<256, 512, LDS_BYTES, stream>>>(x, ckv, wuv, wo, wd, gw, ws, (u16*)d_out);
}
```

```cpp
#include <hip/hip_runtime.h>
#include <hip/hip_bf16.h>
#include <stdint.h>

#define NT_CKV 2
#define NT_WUV 1
#define NT_WO 1
#define NT_WQ 1
#define NT_WUK 1
#define ZERO_AUX 16
#define NT_FFN 1

typedef __attribute__((ext_vector_type(8))) short bf16x8;
typedef __attribute__((ext_vector_type(4))) float f32x4;
typedef __attribute__((ext_vector_type(16))) float f32x16;
typedef __attribute__((ext_vector_type(4))) unsigned u32x4;
typedef __attribute__((ext_vector_type(2))) unsigned u32x2;
typedef unsigned short u16;

#define GAS __attribute__((address_space(1)))
typedef GAS unsigned gu32;
typedef GAS unsigned long long gu64;
#define RLX_AGENT __ATOMIC_RELAXED, __HIP_MEMORY_SCOPE_AGENT

#define WS_EPOCH   0
#define WS_FQ      1024
#define WS_FP      2048
#define WS_FO      3072
#define WS_FF      4096
#define WS_OPART   65536
#define WS_FPART   327680
#define WS_STATS   786432
#define WS_PART    1048576
#define WS_QABS    5242880

#define STAMP(ws, slot) do {} while (0)

__device__ __forceinline__ u16 f2bf(float f) { return __builtin_bit_cast(u16, (__bf16)f); }
__device__ __forceinline__ float bf2f(u16 v) { return __builtin_bit_cast(float, (unsigned)v << 16); }
__device__ __forceinline__ float bfr(float f) { return bf2f(f2bf(f)); }
__device__ __forceinline__ unsigned pack2(float lo, float hi) { return (unsigned)f2bf(lo) | ((unsigned)f2bf(hi) << 16); }
__device__ __forceinline__ float lo_f(unsigned d) { return __builtin_bit_cast(float, d << 16); }
__device__ __forceinline__ float hi_f(unsigned d) { return __builtin_bit_cast(float, d & 0xffff0000u); }
__device__ __forceinline__ unsigned nz(unsigned d) { return d ? d : 0x8000u; }
__device__ __forceinline__ bool allnz(u32x4 v) { return v[0] && v[1] && v[2] && v[3]; }

template <int NT> __device__ __forceinline__ u32x4 ld16(const void* p) {
  if (NT) return __builtin_nontemporal_load((const u32x4*)p);
  return *(const u32x4*)p;
}

template <int CTRL> __device__ __forceinline__ float dpp(float x) {
  return __builtin_bit_cast(float, __builtin_amdgcn_mov_dpp(__builtin_bit_cast(int, x), CTRL, 0xf, 0xf, true));
}
#define XOR1 0xB1
#define XOR2 0x4E
#define ROR4 0x124
#define ROR8 0x128
__device__ __forceinline__ float x16_max(float x) {
  auto s = __builtin_amdgcn_permlane16_swap(__float_as_uint(x), __float_as_uint(x), false, false);
  return fmaxf(__uint_as_float(s[0]), __uint_as_float(s[1]));
}
__device__ __forceinline__ float x32_max(float x) {
  auto s = __builtin_amdgcn_permlane32_swap(__float_as_uint(x), __float_as_uint(x), false, false);
  return fmaxf(__uint_as_float(s[0]), __uint_as_float(s[1]));
}
__device__ __forceinline__ float x16_sum(float x) {
  auto s = __builtin_amdgcn_permlane16_swap(__float_as_uint(x), __float_as_uint(x), false, false);
  return __uint_as_float(s[0]) + __uint_as_float(s[1]);
}
__device__ __forceinline__ float x32_sum(float x) {
  auto s = __builtin_amdgcn_permlane32_swap(__float_as_uint(x), __float_as_uint(x), false, false);
  return __uint_as_float(s[0]) + __uint_as_float(s[1]);
}

__device__ __forceinline__ unsigned fsw(unsigned r) { return ((r & 3u) << 2) | ((0x78u >> (2u * ((r >> 2) & 3u))) & 3u); }

__device__ __forceinline__ bf16x8 as_frag(u32x4 v) { return __builtin_bit_cast(bf16x8, v); }

__device__ __forceinline__ bf16x8 colfrag(const u32x4 (&r)[8], int i) {
  const int u = i >> 1;
  const unsigned sel = (i & 1) ? 0x07060302u : 0x05040100u;
  u32x4 o;
  o[0] = __builtin_amdgcn_perm(r[1][u], r[0][u], sel);
  o[1] = __builtin_amdgcn_perm(r[3][u], r[2][u], sel);
  o[2] = __builtin_amdgcn_perm(r[5][u], r[4][u], sel);
  o[3] = __builtin_amdgcn_perm(r[7][u], r[6][u], sel);
  return as_frag(o);
}

#define LDS_BYTES 163840
#define SPIN_CAP (1u << 16)
#define RAW_BARRIER() do { asm volatile("s_waitcnt lgkmcnt(0)" ::: "memory"); __builtin_amdgcn_s_barrier(); asm volatile("" ::: "memory"); } while (0)
#define DRAIN() asm volatile("s_waitcnt vmcnt(0)" ::: "memory")
#define GLDS16(src, dst, aux) __builtin_amdgcn_global_load_lds((const unsigned*)(src), (__attribute__((address_space(3))) unsigned*)(dst), 16, 0, (aux))
#define TR_READ(dst, addr, off) asm volatile("ds_read_b64_tr_b16 %0, %1 offset:%2" : "=v"(dst) : "v"(addr), "i"(off) : "memory")
#define B128_READ(dst, addr) asm volatile("ds_read_b128 %0, %1" : "=v"(dst) : "v"(addr) : "memory")
typedef unsigned long long u64;

__device__ __forceinline__ void attention_role(const u16* __restrict__ ckv, unsigned char* __restrict__ ws,
                                               unsigned char* smem, int u) {
  const int tid = threadIdx.x, l = tid & 63, g = l >> 4, ch = l & 15;
  const int w = __builtin_amdgcn_readfirstlane(tid >> 6);
  const int b = u >> 5, chunk = u & 31;
  unsigned char* sC = smem;
  unsigned char* sQ = smem + 131072;
  STAMP(ws, 0);
  const u64 tag = __hip_atomic_load((gu64*)(ws + WS_EPOCH), RLX_AGENT) + 1ull;
  {
    const u16* crow = ckv + (size_t)(b * 4096 + chunk * 128) * 512;
#pragma unroll
    for (int p = 0; p < 4; ++p)
#pragma unroll
      for (int rg = 0; rg < 4; ++rg) {
        const int r = 16 * w + 4 * rg + g;
        const unsigned cc = (unsigned)ch ^ fsw(r & 15);
        GLDS16(crow + (size_t)r * 512 + p * 128 + cc * 8, sC + p * 32768 + (16 * w + 4 * rg) * 256, NT_CKV);
      }
  }
  {
    gu64* fq = (gu64*)(ws + WS_FQ) + 16 * w + (l & 15);
    for (unsigned spins = 0; spins < SPIN_CAP; ++spins) {
      const u64 f = __hip_atomic_load(fq, RLX_AGENT);
      if (__all(f == tag)) break;
      __builtin_amdgcn_s_sleep(1);
    }
    STAMP(ws, 1);
    const __amdgpu_buffer_rsrc_t qrs = __builtin_amdgcn_make_buffer_rsrc(ws + WS_QABS, 0, 131072, 0x00020000);
    u32x4 qv[4];
#pragma unroll
    for (int i = 0; i < 4; ++i) qv[i] = __builtin_amdgcn_raw_buffer_load_b128(qrs, (unsigned)((b * 32 + 4 * w + i) * 1024 + l * 16), 0, 16);
#pragma unroll
    for (int i = 0; i < 4; ++i) {
      const int hd = 4 * w + i;
      *(u32x4*)(sQ + hd * 1024 + ((l & ~15u) | ((l & 15u) ^ fsw(hd & 15))) * 16) = qv[i];
    }
  }
  RAW_BARRIER();
  u32x4 qf[2][16];
  const unsigned fs = fsw(ch);
#pragma unroll
  for (int t = 0; t < 2; ++t)
#pragma unroll
    for (int s = 0; s < 16; ++s) {
      const unsigned c64 = 4 * s + g;
      qf[t][s] = *(const u32x4*)(sQ + (16 * t + ch) * 1024 + ((c64 & ~15u) | ((c64 & 15u) ^ fs)) * 16);
    }
  RAW_BARRIER();
  STAMP(ws, 2);
  DRAIN();
  f32x4 s0 = (f32x4){0.f, 0.f, 0.f, 0.f}, s1 = (f32x4){0.f, 0.f, 0.f, 0.f};
#pragma unroll
  for (int p = 0; p < 4; ++p) {
    u32x4 a[4];
#pragma unroll
    for (int ss = 0; ss < 4; ++ss)
      a[ss] = *(const u32x4*)(sC + p * 32768 + (16 * w + ch) * 256 + ((unsigned)(4 * ss + g) ^ fs) * 16);
#pragma unroll
    for (int ss = 0; ss < 4; ++ss) {
      s0 = __builtin_amdgcn_mfma_f32_16x16x32_bf16(as_frag(a[ss]), as_frag(qf[0][4 * p + ss]), s0, 0, 0, 0);
      s1 = __builtin_amdgcn_mfma_f32_16x16x32_bf16(as_frag(a[ss]), as_frag(qf[1][4 * p + ss]), s1, 0, 0, 0);
    }
  }
  STAMP(ws, 3);
  unsigned char* sP = sQ;
  float* wmax = (float*)(sQ + 8192);
  float* wsum = (float*)(sQ + 9216);
  {
    float m0 = fmaxf(fmaxf(s0[0], s0[1]), fmaxf(s0[2], s0[3]));
    float m1 = fmaxf(fmaxf(s1[0], s1[1]), fmaxf(s1[2], s1[3]));
    m0 = x32_max(x16_max(m0)); m1 = x32_max(x16_max(m1));
    if (g == 0) wmax[ch * 8 + w] = m0;
    if (g == 1) wmax[(16 + ch) * 8 + w] = m1;
  }
  RAW_BARRIER();
  const float cs = 0.08838834764831845f * 1.4426950408889634f;
  {
    const f32x4 a0 = *(const f32x4*)&wmax[ch * 8], a1 = *(const f32x4*)&wmax[ch * 8 + 4];
    const f32x4 b0 = *(const f32x4*)&wmax[(16 + ch) * 8], b1 = *(const f32x4*)&wmax[(16 + ch) * 8 + 4];
    const float M0 = fmaxf(fmaxf(fmaxf(a0[0], a0[1]), fmaxf(a0[2], a0[3])), fmaxf(fmaxf(a1[0], a1[1]), fmaxf(a1[2], a1[3])));
    const float M1 = fmaxf(fmaxf(fmaxf(b0[0], b0[1]), fmaxf(b0[2], b0[3])), fmaxf(fmaxf(b1[0], b1[1]), fmaxf(b1[2], b1[3])));
    u32x2 p0, p1;
    p0[0] = pack2(exp2f((s0[0] - M0) * cs), exp2f((s0[1] - M0) * cs));
    p0[1] = pack2(exp2f((s0[2] - M0) * cs), exp2f((s0[3] - M0) * cs));
    p1[0] = pack2(exp2f((s1[0] - M1) * cs), exp2f((s1[1] - M1) * cs));
    p1[1] = pack2(exp2f((s1[2] - M1) * cs), exp2f((s1[3] - M1) * cs));
    const unsigned off = (((unsigned)(2 * w + (g >> 1)) ^ fs) * 16) + (g & 1) * 8;
    *(u32x2*)(sP + ch * 256 + off) = p0;
    *(u32x2*)(sP + (16 + ch) * 256 + off) = p1;
    float l0 = (lo_f(p0[0]) + hi_f(p0[0])) + (lo_f(p0[1]) + hi_f(p0[1]));
    float l1 = (lo_f(p1[0]) + hi_f(p1[0])) + (lo_f(p1[1]) + hi_f(p1[1]));
    l0 = x32_sum(x16_sum(l0)); l1 = x32_sum(x16_sum(l1));
    if (g == 0) wsum[ch * 8 + w] = l0;
    if (g == 1) wsum[(16 + ch) * 8 + w] = l1;
  }
  RAW_BARRIER();
  STAMP(ws, 4);
  const int hd32 = l & 31, hh = l >> 5;
  f32x16 o0, o1;
#pragma unroll
  for (int i = 0; i < 16; ++i) { o0[i] = 0.f; o1[i] = 0.f; }
  {
    const unsigned blk = (l >> 4) & 1, q = (l & 15) >> 2, p4 = l & 3;
    const unsigned sCbase = (unsigned)(size_t)(sC) + (w >> 1) * 32768;
    const unsigned sPbase = (unsigned)(size_t)(sP) + hd32 * 256;
    const unsigned fh = fsw(hd32 & 15);
    unsigned ta[2][2];
#pragma unroll
    for (int tt = 0; tt < 2; ++tt)
#pragma unroll
      for (int t2 = 0; t2 < 2; ++t2) {
        const unsigned row = 8 * hh + 4 * t2 + q;
        const unsigned cc = 4 * (2 * (w & 1) + tt) + 2 * blk + (p4 >> 1);
        ta[tt][t2] = sCbase + row * 256 + ((cc ^ fsw(row)) * 16) + 8 * (p4 & 1);
      }
    u32x4 pb[8]; u32x2 a[8][2][2];
#define PV_ISSUE(ks) do { B128_READ(pb[ks], sPbase + (((unsigned)(2 * (ks) + hh)) ^ fh) * 16); \
      TR_READ(a[ks][0][0], ta[0][0], (ks) * 4096); TR_READ(a[ks][0][1], ta[0][1], (ks) * 4096); \
      TR_READ(a[ks][1][0], ta[1][0], (ks) * 4096); TR_READ(a[ks][1][1], ta[1][1], (ks) * 4096); } while (0)
#define PV_WAIT(ks, n) asm volatile("s_waitcnt lgkmcnt(" #n ")" : "+v"(pb[ks]), "+v"(a[ks][0][0]), "+v"(a[ks][0][1]), "+v"(a[ks][1][0]), "+v"(a[ks][1][1]) :: "memory")
#define PV_MMA(ks) do { \
      u32x4 a0 = (u32x4){a[ks][0][0][0], a[ks][0][0][1], a[ks][0][1][0], a[ks][0][1][1]}; \
      u32x4 a1 = (u32x4){a[ks][1][0][0], a[ks][1][0][1], a[ks][1][1][0], a[ks][1][1][1]}; \
      o0 = __builtin_amdgcn_mfma_f32_32x32x16_bf16(as_frag(a0), as_frag(pb[ks]), o0, 0, 0, 0); \
      o1 = __builtin_amdgcn_mfma_f32_32x32x16_bf16(as_frag(a1), as_frag(pb[ks]), o1, 0, 0, 0); } while (0)
    PV_ISSUE(0); PV_ISSUE(1); PV_ISSUE(2);
    PV_WAIT(0, 10); PV_MMA(0); __builtin_amdgcn_sched_barrier(0); PV_ISSUE(3);
    PV_WAIT(1, 10); PV_MMA(1); __builtin_amdgcn_sched_barrier(0); PV_ISSUE(4);
    PV_WAIT(2, 10); PV_MMA(2); __builtin_amdgcn_sched_barrier(0); PV_ISSUE(5);
    PV_WAIT(3, 10); PV_MMA(3); __builtin_amdgcn_sched_barrier(0); PV_ISSUE(6);
    PV_WAIT(4, 10); PV_MMA(4); __builtin_amdgcn_sched_barrier(0); PV_ISSUE(7);
    PV_WAIT(5, 10); PV_MMA(5);
    PV_WAIT(6, 5);  PV_MMA(6);
    PV_WAIT(7, 0);  PV_MMA(7);
  }
  RAW_BARRIER();
  STAMP(ws, 5);
  {
    unsigned char* st = sC + w * 4096;
#pragma unroll
    for (int tt = 0; tt < 2; ++tt)
#pragma unroll
      for (int qq = 0; qq < 4; ++qq) {
        u32x2 v;
        if (tt == 0) { v[0] = pack2(o0[4 * qq], o0[4 * qq + 1]); v[1] = pack2(o0[4 * qq + 2], o0[4 * qq + 3]); }
        else         { v[0] = pack2(o1[4 * qq], o1[4 * qq + 1]); v[1] = pack2(o1[4 * qq + 2], o1[4 * qq + 3]); }
        *(u32x2*)(st + hd32 * 128 + (((unsigned)(4 * tt + qq) ^ (unsigned)(hd32 & 7)) * 16) + 8 * hh) = v;
      }
    const __amdgpu_buffer_rsrc_t prs = __builtin_amdgcn_make_buffer_rsrc(ws + WS_PART, 0, 128 * 32768, 0x00020000);
#pragma unroll
    for (int n = 0; n < 4; ++n) {
      const int hd = 8 * n + (l >> 3);
      const u32x4 v = *(const u32x4*)(st + hd * 128 + (((unsigned)(l & 7) ^ (unsigned)(hd & 7)) * 16));
      __builtin_amdgcn_raw_buffer_store_b128(v, prs, (unsigned)((u * 32 + hd) * 1024 + w * 128 + (l & 7) * 16), 0, 16);
    }
    if (tid < 32) {
      const f32x4 a0 = *(const f32x4*)&wmax[tid * 8], a1 = *(const f32x4*)&wmax[tid * 8 + 4];
      const f32x4 b0 = *(const f32x4*)&wsum[tid * 8], b1 = *(const f32x4*)&wsum[tid * 8 + 4];
      const float m = fmaxf(fmaxf(fmaxf(a0[0], a0[1]), fmaxf(a0[2], a0[3])), fmaxf(fmaxf(a1[0], a1[1]), fmaxf(a1[2], a1[3])));
      const float s = ((b0[0] + b0[1]) + (b0[2] + b0[3])) + ((b1[0] + b1[1]) + (b1[2] + b1[3]));
      const u64 v = (u64)__float_as_uint(m * cs) | ((u64)__float_as_uint(s) << 32);
      __hip_atomic_store((gu64*)(ws + WS_STATS) + (b * 32 + tid) * 32 + chunk, v, RLX_AGENT);
    }
  }
  DRAIN();
  RAW_BARRIER();
  if (tid == 0) __hip_atomic_store((gu64*)(ws + WS_FP) + u, tag, RLX_AGENT);
  STAMP(ws, 6);
}

__device__ __forceinline__ void c_role(const u16* __restrict__ x, const u16* __restrict__ wq, const u16* __restrict__ wuk,
                                       const u16* __restrict__ wuv, const u16* __restrict__ wo,
                                       const u16* __restrict__ wg, const u16* __restrict__ wu, const u16* __restrict__ wd,
                                       const u16* __restrict__ gw, unsigned char* __restrict__ ws,
                                       u16* __restrict__ out, unsigned char* smem, int v) {
  const int tid = threadIdx.x, l = tid & 63, g = l >> 4, ch = l & 15;
  const int w = __builtin_amdgcn_readfirstlane(tid >> 6);
  const int h = (v & 7) + 8 * ((v >> 3) & 3), b = v >> 5;
  float* red = (float*)(smem);
  u16* qs = (u16*)(smem + 32768);
  u16* hs = (u16*)(smem + 33792);
  u16* cc = (u16*)(smem + 34816);
  float* red2 = (float*)(smem + 36864);
  float* red3 = (float*)(smem + 40960);
  STAMP(ws, 0);
  const u64 tag = __hip_atomic_load((gu64*)(ws + WS_EPOCH), RLX_AGENT) + 1ull;
  {
    const int c0 = b * 128;
    u32x4 r[2][8];
#pragma unroll
    for (int kb = 0; kb < 2; ++kb)
#pragma unroll
      for (int j = 0; j < 8; ++j)
        r[kb][j] = ld16<NT_WQ>(wq + (size_t)(64 * w + 32 * kb + 8 * g + j) * 4096 + h * 128 + ch * 8);
    u32x4 xa[2];
#pragma unroll
    for (int kb = 0; kb < 2; ++kb) {
      xa[kb] = (u32x4){0u, 0u, 0u, 0u};
      if (ch < 4) xa[kb] = *(const u32x4*)(x + ch * 512 + 64 * w + 32 * kb + 8 * g);
    }
    u32x4 uk[4];
#pragma unroll
    for (int s = 0; s < 4; ++s)
      uk[s] = ld16<NT_WUK>(wuk + (size_t)(c0 + 16 * w + ch) * 4096 + h * 128 + 32 * s + 8 * g);
    f32x4 acc[8];
#pragma unroll
    for (int i = 0; i < 8; ++i) acc[i] = (f32x4){0.f, 0.f, 0.f, 0.f};
#pragma unroll
    for (int kb = 0; kb < 2; ++kb)
#pragma unroll
      for (int i = 0; i < 8; ++i)
        acc[i] = __builtin_amdgcn_mfma_f32_16x16x32_bf16(as_frag(xa[kb]), colfrag(r[kb], i), acc[i], 0, 0, 0);
    if (l < 16) {
#pragma unroll
      for (int bb = 0; bb < 4; ++bb) {
        *(f32x4*)&red[(w * 4 + bb) * 128 + 8 * ch]     = (f32x4){acc[0][bb], acc[1][bb], acc[2][bb], acc[3][bb]};
        *(f32x4*)&red[(w * 4 + bb) * 128 + 8 * ch + 4] = (f32x4){acc[4][bb], acc[5][bb], acc[6][bb], acc[7][bb]};
      }
    }
    __syncthreads();
    {
      float s = 0.f;
#pragma unroll
      for (int ww = 0; ww < 8; ++ww) s += red[ww * 512 + tid];
      qs[tid] = f2bf(s);
    }
    __syncthreads();
    f32x4 a2 = (f32x4){0.f, 0.f, 0.f, 0.f};
#pragma unroll
    for (int s = 0; s < 4; ++s) {
      u32x4 bq = (u32x4){0u, 0u, 0u, 0u};
      if (ch < 4) bq = *(const u32x4*)&qs[ch * 128 + 32 * s + 8 * g];
      a2 = __builtin_amdgcn_mfma_f32_16x16x32_bf16(as_frag(uk[s]), as_frag(bq), a2, 0, 0, 0);
    }
    if (ch < 4) {
      const u64 o = (u64)pack2(a2[0], a2[1]) | ((u64)pack2(a2[2], a2[3]) << 32);
      __hip_atomic_store((gu64*)(ws + WS_QABS) + ((ch * 32 + h) * 512 + c0 + 16 * w + 4 * g) / 4, o, RLX_AGENT);
    }
    DRAIN();
    __syncthreads();
    if (tid == 0) __hip_atomic_store((gu64*)(ws + WS_FQ) + h * 4 + b, tag, RLX_AGENT);
  }
  STAMP(ws, 1);
  if (v < 56) {
    const int f0 = v * 32, half = ch >> 3;
    const u16* base = ((ch & 4) ? wu : wg) + f0 + 8 * (ch & 3);
    u32x4 r[8], rd[8];
#pragma unroll
    for (int j = 0; j < 8; ++j) r[j] = ld16<NT_FFN>(base + (size_t)(256 * half + 32 * w + 8 * g + j) * 1792);
    u32x4 xa = (u32x4){0u, 0u, 0u, 0u};
    if (ch < 8) xa = *(const u32x4*)(x + (ch & 3) * 512 + 256 * (ch >> 2) + 32 * w + 8 * g);
    if (w < 4) {
#pragma unroll
      for (int j = 0; j < 8; ++j) rd[j] = ld16<NT_FFN>(wd + (size_t)(f0 + 8 * g + j) * 512 + 128 * w + 8 * ch);
    }
    f32x4 acc[8];
#pragma unroll
    for (int i = 0; i < 8; ++i) {
      acc[i] = (f32x4){0.f, 0.f, 0.f, 0.f};
      acc[i] = __builtin_amdgcn_mfma_f32_16x16x32_bf16(as_frag(xa), colfrag(r, i), acc[i], 0, 0, 0);
    }
    if (g == half) {
#pragma unroll
      for (int bb = 0; bb < 4; ++bb) {
        *(f32x4*)&red[((2 * w + half) * 4 + bb) * 128 + 8 * (ch & 7)]     = (f32x4){acc[0][bb], acc[1][bb], acc[2][bb], acc[3][bb]};
        *(f32x4*)&red[((2 * w + half) * 4 + bb) * 128 + 8 * (ch & 7) + 4] = (f32x4){acc[4][bb], acc[5][bb], acc[6][bb], acc[7][bb]};
      }
    }
    __syncthreads();
    if (tid < 128) {
      const int bb = tid >> 5, d = tid & 31;
      float gs = 0.f, us = 0.f;
#pragma unroll
      for (int ww = 0; ww < 16; ++ww) { gs += red[(ww * 4 + bb) * 128 + d]; us += red[(ww * 4 + bb) * 128 + d + 32]; }
      gs = bfr(gs); us = bfr(us);
      hs[bb * 32 + d] = f2bf((gs > 0.f ? gs : 0.f) * us);
    }
    __syncthreads();
    if (w < 4) {
      u32x4 ha = (u32x4){0u, 0u, 0u, 0u};
      if (ch < 4) ha = *(const u32x4*)&hs[ch * 32 + 8 * g];
      const __amdgpu_buffer_rsrc_t frs = __builtin_amdgcn_make_buffer_rsrc(ws + WS_FPART, 0, 56 * 8192, 0x00020000);
#pragma unroll
      for (int i = 0; i < 8; ++i) {
        acc[i] = (f32x4){0.f, 0.f, 0.f, 0.f};
        acc[i] = __builtin_amdgcn_mfma_f32_16x16x32_bf16(as_frag(ha), colfrag(rd, i), acc[i], 0, 0, 0);
      }
      if (l < 16) {
#pragma unroll
        for (int bb = 0; bb < 4; ++bb) {
          const unsigned off = (unsigned)(((v * 4 + bb) * 512 + 128 * w + 8 * ch) * 4);
          __builtin_amdgcn_raw_buffer_store_b128((u32x4){__float_as_uint(acc[0][bb]), __float_as_uint(acc[1][bb]), __float_as_uint(acc[2][bb]), __float_as_uint(acc[3][bb])}, frs, off, 0, 16);
          __builtin_amdgcn_raw_buffer_store_b128((u32x4){__float_as_uint(acc[4][bb]), __float_as_uint(acc[5][bb]), __float_as_uint(acc[6][bb]), __float_as_uint(acc[7][bb])}, frs, off + 16, 0, 16);
        }
      }
    }
    DRAIN();
    __syncthreads();
    if (tid == 0) __hip_atomic_store((gu64*)(ws + WS_FF) + v, tag, RLX_AGENT);
  }
  u32x4 r[2][8], ro[2][8];
#pragma unroll
  for (int kb = 0; kb < 2; ++kb)
#pragma unroll
    for (int j = 0; j < 8; ++j)
      r[kb][j] = ld16<NT_WUV>(wuv + (size_t)(64 * w + 32 * kb + 8 * g + j) * 4096 + h * 128 + ch * 8);
#pragma unroll
  for (int cg = 0; cg < 2; ++cg)
#pragma unroll
    for (int j = 0; j < 8; ++j)
      ro[cg][j] = ld16<NT_WO>(wo + (size_t)(h * 128 + 32 * (w >> 1) + 8 * g + j) * 512 + 128 * (2 * (w & 1) + cg) + ch * 8);
  float gate = 0.f, f[4] = {0.f, 0.f, 0.f, 0.f};
  if (v < 64 && w == 7) {
    const int rb = v >> 4, cs0 = (v & 15) * 32;
    const int i8 = l >> 3, c4 = l & 7;
    const u32x4 xv = *(const u32x4*)(x + rb * 512 + 8 * l), gv = *(const u32x4*)(gw + 8 * l);
    float gs = 0.f;
#pragma unroll
    for (int j = 0; j < 4; ++j) { gs += bfr(lo_f(xv[j]) * lo_f(gv[j])); gs += bfr(hi_f(xv[j]) * hi_f(gv[j])); }
    gs += dpp<XOR1>(gs); gs += dpp<XOR2>(gs); gs += dpp<ROR4>(gs); gs += dpp<ROR8>(gs);
    gs = x32_sum(x16_sum(gs));
    gate = bfr(1.f / (1.f + __expf(-bfr(gs))));
    gu64* ff = (gu64*)(ws + WS_FF) + (l < 56 ? l : 0);
    for (unsigned spins = 0; spins < SPIN_CAP; ++spins) {
      const u64 fl = __hip_atomic_load(ff, RLX_AGENT);
      if (__all(fl == tag)) break;
      __builtin_amdgcn_s_sleep(1);
    }
    const __amdgpu_buffer_rsrc_t frs = __builtin_amdgcn_make_buffer_rsrc(ws + WS_FPART, 0, 56 * 8192, 0x00020000);
    u32x4 fv[7];
#pragma unroll
    for (int n = 0; n < 7; ++n)
      fv[n] = __builtin_amdgcn_raw_buffer_load_b128(frs, (unsigned)((((8 * n + i8) * 4 + rb) * 512 + cs0 + 4 * c4) * 4), 0, 16);
#pragma unroll
    for (int j = 0; j < 4; ++j) {
      f[j] = 0.f;
#pragma unroll
      for (int n = 0; n < 7; ++n) f[j] += __uint_as_float(fv[n][j]);
      f[j] += dpp<ROR8>(f[j]); f[j] = x32_sum(x16_sum(f[j]));
    }
  }
  STAMP(ws, 2);
  {
    gu64* fp = (gu64*)(ws + WS_FP) + b * 32 + (l & 31);
    for (unsigned spins = 0; spins < SPIN_CAP; ++spins) {
      const u64 fl = __hip_atomic_load(fp, RLX_AGENT);
      if (__all(fl == tag)) break;
      __builtin_amdgcn_s_sleep(1);
    }
    STAMP(ws, 3);
    const __amdgpu_buffer_rsrc_t prs = __builtin_amdgcn_make_buffer_rsrc(ws + WS_PART, 0, 128 * 32768, 0x00020000);
    u32x4 pv[4];
#pragma unroll
    for (int n = 0; n < 4; ++n)
      pv[n] = __builtin_amdgcn_raw_buffer_load_b128(prs, (unsigned)(((b * 32 + 8 * n + (l >> 3)) * 32 + h) * 1024 + w * 128 + (l & 7) * 16), 0, 16);
    const u64 st = __hip_atomic_load((gu64*)(ws + WS_STATS) + (b * 32 + h) * 32 + (l & 31), RLX_AGENT);
    const float m = __uint_as_float((unsigned)st), ls = __uint_as_float((unsigned)(st >> 32));
    float M = m;
    M = fmaxf(M, dpp<XOR1>(M)); M = fmaxf(M, dpp<XOR2>(M)); M = fmaxf(M, dpp<ROR4>(M)); M = fmaxf(M, dpp<ROR8>(M));
    M = x16_max(M);
    const float wt = exp2f(m - M);
    float L = wt * ls;
    L += dpp<XOR1>(L); L += dpp<XOR2>(L); L += dpp<ROR4>(L); L += dpp<ROR8>(L);
    L = x16_sum(L);
    const float inv = 1.f / L;
    float a[8];
#pragma unroll
    for (int i = 0; i < 8; ++i) a[i] = 0.f;
#pragma unroll
    for (int n = 0; n < 4; ++n) {
      const float wi = __shfl(wt, 8 * n + (l >> 3));
#pragma unroll
      for (int j = 0; j < 4; ++j) { a[2 * j] += wi * lo_f(pv[n][j]); a[2 * j + 1] += wi * hi_f(pv[n][j]); }
    }
#pragma unroll
    for (int i = 0; i < 8; ++i) { a[i] += dpp<ROR8>(a[i]); a[i] = x32_sum(x16_sum(a[i])) * inv; }
    if (l < 16) {
      u32x4 o;
#pragma unroll
      for (int j = 0; j < 4; ++j) {
        const unsigned hi = pack2(a[2 * j], a[2 * j + 1]);
        o[j] = (l < 8) ? hi : pack2(a[2 * j] - lo_f(hi), a[2 * j + 1] - hi_f(hi));
      }
      *(u32x4*)&cc[(w * 2 + (l >> 3)) * 64 + 8 * (l & 7)] = o;
    }
  }
  STAMP(ws, 4);
  {
    f32x4 acc[8];
#pragma unroll
    for (int i = 0; i < 8; ++i) acc[i] = (f32x4){0.f, 0.f, 0.f, 0.f};
#pragma unroll
    for (int kb = 0; kb < 2; ++kb) {
      u32x4 xa = (u32x4){0u, 0u, 0u, 0u};
      if (ch < 2) xa = *(const u32x4*)&cc[(w * 2 + ch) * 64 + 32 * kb + 8 * g];
#pragma unroll
      for (int i = 0; i < 8; ++i) acc[i] = __builtin_amdgcn_mfma_f32_16x16x32_bf16(as_frag(xa), colfrag(r[kb], i), acc[i], 0, 0, 0);
    }
    if (l < 16) {
      *(f32x4*)&red2[w * 128 + 8 * ch]     = (f32x4){acc[0][0] + acc[0][1], acc[1][0] + acc[1][1], acc[2][0] + acc[2][1], acc[3][0] + acc[3][1]};
      *(f32x4*)&red2[w * 128 + 8 * ch + 4] = (f32x4){acc[4][0] + acc[4][1], acc[5][0] + acc[5][1], acc[6][0] + acc[6][1], acc[7][0] + acc[7][1]};
    }
  }
  __syncthreads();
  u32x4 xa = (u32x4){0u, 0u, 0u, 0u};
  {
    float s[8];
#pragma unroll
    for (int j = 0; j < 8; ++j) s[j] = 0.f;
    const int d0 = 32 * (w >> 1) + 8 * g;
#pragma unroll
    for (int ww = 0; ww < 8; ++ww) {
      const f32x4 u0 = *(const f32x4*)&red2[ww * 128 + d0], u1 = *(const f32x4*)&red2[ww * 128 + d0 + 4];
      s[0] += u0[0]; s[1] += u0[1]; s[2] += u0[2]; s[3] += u0[3]; s[4] += u1[0]; s[5] += u1[1]; s[6] += u1[2]; s[7] += u1[3];
    }
    if (ch == 0) xa = (u32x4){pack2(s[0], s[1]), pack2(s[2], s[3]), pack2(s[4], s[5]), pack2(s[6], s[7])};
  }
#pragma unroll
  for (int cg = 0; cg < 2; ++cg) {
    f32x4 acc[8];
#pragma unroll
    for (int i = 0; i < 8; ++i) {
      acc[i] = (f32x4){0.f, 0.f, 0.f, 0.f};
      acc[i] = __builtin_amdgcn_mfma_f32_16x16x32_bf16(as_frag(xa), colfrag(ro[cg], i), acc[i], 0, 0, 0);
    }
    if (l < 16) {
      float* d = &red3[(w >> 1) * 512 + 128 * (2 * (w & 1) + cg) + 8 * ch];
      *(f32x4*)d = (f32x4){acc[0][0], acc[1][0], acc[2][0], acc[3][0]};
      *(f32x4*)(d + 4) = (f32x4){acc[4][0], acc[5][0], acc[6][0], acc[7][0]};
    }
  }
  __syncthreads();
  if (w < 2) {
    const f32x4 u0 = *(const f32x4*)&red3[4 * tid], u1 = *(const f32x4*)&red3[512 + 4 * tid];
    const f32x4 u2 = *(const f32x4*)&red3[1024 + 4 * tid], u3 = *(const f32x4*)&red3[1536 + 4 * tid];
    const __amdgpu_buffer_rsrc_t ors = __builtin_amdgcn_make_buffer_rsrc(ws + WS_OPART, 0, 128 * 2048, 0x00020000);
    u32x4 o;
#pragma unroll
    for (int j = 0; j < 4; ++j) o[j] = __float_as_uint((u0[j] + u1[j]) + (u2[j] + u3[j]));
    __builtin_amdgcn_raw_buffer_store_b128(o, ors, (unsigned)(((b * 32 + h) * 512 + 4 * tid) * 4), 0, 16);
    DRAIN();
  }
  __syncthreads();
  if (tid == 0) __hip_atomic_store((gu64*)(ws + WS_FO) + b * 32 + h, tag, RLX_AGENT);
  STAMP(ws, 5);
  if (v < 64 && w == 7) {
    const int rb = v >> 4, cs0 = (v & 15) * 32;
    const int i8 = l >> 3, c4 = l & 7;
    gu64* fo = (gu64*)(ws + WS_FO) + rb * 32 + (l & 31);
    for (unsigned spins = 0; spins < SPIN_CAP; ++spins) {
      const u64 fl = __hip_atomic_load(fo, RLX_AGENT);
      if (__all(fl == tag)) break;
      __builtin_amdgcn_s_sleep(1);
    }
    const __amdgpu_buffer_rsrc_t ors = __builtin_amdgcn_make_buffer_rsrc(ws + WS_OPART, 0, 128 * 2048, 0x00020000);
    u32x4 av[4];
#pragma unroll
    for (int n = 0; n < 4; ++n)
      av[n] = __builtin_amdgcn_raw_buffer_load_b128(ors, (unsigned)(((rb * 32 + 8 * n + i8) * 512 + cs0 + 4 * c4) * 4), 0, 16);
    float res[4];
#pragma unroll
    for (int j = 0; j < 4; ++j) {
      float a = (__uint_as_float(av[0][j]) + __uint_as_float(av[1][j])) + (__uint_as_float(av[2][j]) + __uint_as_float(av[3][j]));
      a += dpp<ROR8>(a); a = x32_sum(x16_sum(a));
      res[j] = bfr(gate * bfr(a)) + bfr(bfr(1.f - gate) * bfr(f[j]));
    }
    if (l < 8) *(u32x2*)(out + rb * 512 + cs0 + 4 * l) = (u32x2){pack2(res[0], res[1]), pack2(res[2], res[3])};
    if (v == 0) {
      gu64* fa = (gu64*)(ws + WS_FO) + l;
      for (unsigned spins = 0; spins < SPIN_CAP; ++spins) {
        const u64 f0 = __hip_atomic_load(fa, RLX_AGENT), f1 = __hip_atomic_load(fa + 64, RLX_AGENT);
        if (__all(f0 == tag && f1 == tag)) break;
        __builtin_amdgcn_s_sleep(1);
      }
      if (l == 0) __hip_atomic_store((gu64*)(ws + WS_EPOCH), tag, RLX_AGENT);
    }
  }
}

__global__ __launch_bounds__(512, 2) void fused_kernel(const u16* __restrict__ x, const u16* __restrict__ ckv,
                                                       const u16* __restrict__ wq, const u16* __restrict__ wuk,
                                                       const u16* __restrict__ wuv, const u16* __restrict__ wo,
                                                       const u16* __restrict__ wg, const u16* __restrict__ wu,
                                                       const u16* __restrict__ wd, const u16* __restrict__ gw,
                                                       unsigned char* __restrict__ ws, u16* __restrict__ out) {
  extern __shared__ __attribute__((aligned(16))) unsigned char smem[];
  const int bid = blockIdx.x;
  if (bid < 128) attention_role(ckv, ws, smem, bid);
  else c_role(x, wq, wuk, wuv, wo, wg, wu, wd, gw, ws, out, smem, bid - 128);
}

extern "C" void kernel_launch(void* const* d_in, const int* in_sizes, int n_in,
                              void* d_out, int out_size, void* d_ws, size_t ws_size,
                              hipStream_t stream) {
  const u16* x   = (const u16*)d_in[0];
  const u16* ckv = (const u16*)d_in[1];
  const u16* wq  = (const u16*)d_in[2];
  const u16* wuk = (const u16*)d_in[3];
  const u16* wuv = (const u16*)d_in[4];
  const u16* wo  = (const u16*)d_in[5];
  const u16* wg  = (const u16*)d_in[7];
  const u16* wu  = (const u16*)d_in[8];
  const u16* wd  = (const u16*)d_in[9];
  const u16* gw  = (const u16*)d_in[10];
  (void)hipFuncSetAttribute((const void*)fused_kernel, hipFuncAttributeMaxDynamicSharedMemorySize, LDS_BYTES);
  fused_kernel<<<256, 512, LDS_BYTES, stream>>>(x, ckv, wq, wuk, wuv, wo, wg, wu, wd, gw, (unsigned char*)d_ws, (u16*)d_out);
}
```

```cpp
#include <hip/hip_runtime.h>
#include <hip/hip_bf16.h>
#include <stdint.h>

#define NT_CKV 2
#define NT_WUV 1
#define NT_WO 1
#define NT_WQ 1
#define NT_WUK 1
#define ZERO_AUX 16
#define NT_FFN 1

typedef __attribute__((ext_vector_type(8))) short bf16x8;
typedef __attribute__((ext_vector_type(4))) float f32x4;
typedef __attribute__((ext_vector_type(16))) float f32x16;
typedef __attribute__((ext_vector_type(4))) unsigned u32x4;
typedef __attribute__((ext_vector_type(2))) unsigned u32x2;
typedef unsigned short u16;

#define GAS __attribute__((address_space(1)))
typedef GAS unsigned gu32;
typedef GAS unsigned long long gu64;
#define RLX_AGENT __ATOMIC_RELAXED, __HIP_MEMORY_SCOPE_AGENT

#define WS_OPART   0
#define WS_FPART   262144
#define WS_STATS   720896
#define WS_PART    753664
#define WS_ZERO_BYTES (753664 + 4194304)
#define WS_QABS    5242880
#define WS_HBUF    5505024

__device__ __forceinline__ u16 f2bf(float f) { return __builtin_bit_cast(u16, (__bf16)f); }
__device__ __forceinline__ float bf2f(u16 v) { return __builtin_bit_cast(float, (unsigned)v << 16); }
__device__ __forceinline__ float bfr(float f) { return bf2f(f2bf(f)); }
__device__ __forceinline__ unsigned pack2(float lo, float hi) { return (unsigned)f2bf(lo) | ((unsigned)f2bf(hi) << 16); }
__device__ __forceinline__ float lo_f(unsigned d) { return __builtin_bit_cast(float, d << 16); }
__device__ __forceinline__ float hi_f(unsigned d) { return __builtin_bit_cast(float, d & 0xffff0000u); }
__device__ __forceinline__ unsigned nz(unsigned d) { return d ? d : 0x8000u; }
__device__ __forceinline__ bool allnz(u32x4 v) { return v[0] && v[1] && v[2] && v[3]; }

template <int NT> __device__ __forceinline__ u32x4 ld16(const void* p) {
  if (NT) return __builtin_nontemporal_load((const u32x4*)p);
  return *(const u32x4*)p;
}

template <int CTRL> __device__ __forceinline__ float dpp(float x) {
  return __builtin_bit_cast(float, __builtin_amdgcn_mov_dpp(__builtin_bit_cast(int, x), CTRL, 0xf, 0xf, true));
}
#define XOR1 0xB1
#define XOR2 0x4E
#define ROR4 0x124
#define ROR8 0x128
__device__ __forceinline__ float x16_max(float x) {
  auto s = __builtin_amdgcn_permlane16_swap(__float_as_uint(x), __float_as_uint(x), false, false);
  return fmaxf(__uint_as_float(s[0]), __uint_as_float(s[1]));
}
__device__ __forceinline__ float x32_max(float x) {
  auto s = __builtin_amdgcn_permlane32_swap(__float_as_uint(x), __float_as_uint(x), false, false);
  return fmaxf(__uint_as_float(s[0]), __uint_as_float(s[1]));
}
__device__ __forceinline__ float x16_sum(float x) {
  auto s = __builtin_amdgcn_permlane16_swap(__float_as_uint(x), __float_as_uint(x), false, false);
  return __uint_as_float(s[0]) + __uint_as_float(s[1]);
}
__device__ __forceinline__ float x32_sum(float x) {
  auto s = __builtin_amdgcn_permlane32_swap(__float_as_uint(x), __float_as_uint(x), false, false);
  return __uint_as_float(s[0]) + __uint_as_float(s[1]);
}

__device__ __forceinline__ unsigned fsw(unsigned r) { return ((r & 3u) << 2) | ((0x78u >> (2u * ((r >> 2) & 3u))) & 3u); }

__device__ __forceinline__ bf16x8 as_frag(u32x4 v) { return __builtin_bit_cast(bf16x8, v); }

__device__ __forceinline__ bf16x8 colfrag(const u32x4 (&r)[8], int i) {
  const int u = i >> 1;
  const unsigned sel = (i & 1) ? 0x07060302u : 0x05040100u;
  u32x4 o;
  o[0] = __builtin_amdgcn_perm(r[1][u], r[0][u], sel);
  o[1] = __builtin_amdgcn_perm(r[3][u], r[2][u], sel);
  o[2] = __builtin_amdgcn_perm(r[5][u], r[4][u], sel);
  o[3] = __builtin_amdgcn_perm(r[7][u], r[6][u], sel);
  return as_frag(o);
}

#define K1_FFN_BLOCKS 56
#define K1_ZERO_BLOCKS 72
__global__ __launch_bounds__(512) void k1_kernel(const u16* __restrict__ x, const u16* __restrict__ wq,
                                                 const u16* __restrict__ wuk, const u16* __restrict__ wg,
                                                 const u16* __restrict__ wu, unsigned char* __restrict__ ws) {
  __shared__ __attribute__((aligned(16))) float red[16][4][128];
  __shared__ __attribute__((aligned(16))) u16 qs[4][128];
  const int tid = threadIdx.x, w = tid >> 6, l = tid & 63, g = l >> 4, ch = l & 15;
  const int bid = blockIdx.x;
  if (bid >= 128 + K1_FFN_BLOCKS) {
    const __amdgpu_buffer_rsrc_t zrs = __builtin_amdgcn_make_buffer_rsrc(ws, 0, WS_ZERO_BYTES, 0x00020000);
    const int zi = bid - 128 - K1_FFN_BLOCKS;
#pragma unroll
    for (int i = 0; i < 9; ++i)
      __builtin_amdgcn_raw_buffer_store_b128((u32x4){0u, 0u, 0u, 0u}, zrs, (unsigned)(((i * K1_ZERO_BLOCKS + zi) * 512 + tid) * 16), 0, ZERO_AUX);
    return;
  }
  if (bid >= 128) {
    const int f0 = (bid - 128) * 32, half = ch >> 3;
    const u16* base = ((ch & 4) ? wu : wg) + f0 + 8 * (ch & 3);
    u32x4 r[8];
#pragma unroll
    for (int j = 0; j < 8; ++j) r[j] = ld16<NT_FFN>(base + (size_t)(256 * half + 32 * w + 8 * g + j) * 1792);
    u32x4 xa = (u32x4){0u, 0u, 0u, 0u};
    if (ch < 8) xa = *(const u32x4*)(x + (ch & 3) * 512 + 256 * (ch >> 2) + 32 * w + 8 * g);
    f32x4 acc[8];
#pragma unroll
    for (int i = 0; i < 8; ++i) {
      acc[i] = (f32x4){0.f, 0.f, 0.f, 0.f};
      acc[i] = __builtin_amdgcn_mfma_f32_16x16x32_bf16(as_frag(xa), colfrag(r, i), acc[i], 0, 0, 0);
    }
    if (g == half) {
#pragma unroll
      for (int b = 0; b < 4; ++b) {
        *(f32x4*)&red[2 * w + half][b][8 * (ch & 7)]     = (f32x4){acc[0][b], acc[1][b], acc[2][b], acc[3][b]};
        *(f32x4*)&red[2 * w + half][b][8 * (ch & 7) + 4] = (f32x4){acc[4][b], acc[5][b], acc[6][b], acc[7][b]};
      }
    }
    __syncthreads();
    if (tid < 128) {
      const int b = tid >> 5, d = tid & 31;
      float gs = 0.f, us = 0.f;
#pragma unroll
      for (int ww = 0; ww < 16; ++ww) { gs += red[ww][b][d]; us += red[ww][b][d + 32]; }
      gs = bfr(gs); us = bfr(us);
      const float rg = gs > 0.f ? gs : 0.f;
      ((u16*)(ws + WS_HBUF))[b * 1792 + f0 + d] = f2bf(rg * us);
    }
    return;
  }
  const int qi = bid;
  const int h = (qi & 7) + 8 * ((qi >> 3) & 3);
  const int c0 = (qi >> 5) * 128;
  const u16* base = wq + h * 128 + ch * 8;
  u32x4 r[2][8];
#pragma unroll
  for (int kb = 0; kb < 2; ++kb)
#pragma unroll
    for (int j = 0; j < 8; ++j)
      r[kb][j] = ld16<NT_WQ>(base + (size_t)(64 * w + 32 * kb + 8 * g + j) * 4096);
  u32x4 xa[2];
#pragma unroll
  for (int kb = 0; kb < 2; ++kb) {
    xa[kb] = (u32x4){0u, 0u, 0u, 0u};
    if (ch < 4) xa[kb] = *(const u32x4*)(x + ch * 512 + 64 * w + 32 * kb + 8 * g);
  }
  u32x4 uk[4];
#pragma unroll
  for (int s = 0; s < 4; ++s)
    uk[s] = ld16<NT_WUK>(wuk + (size_t)(c0 + 16 * w + ch) * 4096 + h * 128 + 32 * s + 8 * g);
  f32x4 acc[8];
#pragma unroll
  for (int i = 0; i < 8; ++i) acc[i] = (f32x4){0.f, 0.f, 0.f, 0.f};
#pragma unroll
  for (int kb = 0; kb < 2; ++kb)
#pragma unroll
    for (int i = 0; i < 8; ++i)
      acc[i] = __builtin_amdgcn_mfma_f32_16x16x32_bf16(as_frag(xa[kb]), colfrag(r[kb], i), acc[i], 0, 0, 0);
  if (l < 16) {
#pragma unroll
    for (int b = 0; b < 4; ++b) {
      *(f32x4*)&red[w][b][8 * ch]     = (f32x4){acc[0][b], acc[1][b], acc[2][b], acc[3][b]};
      *(f32x4*)&red[w][b][8 * ch + 4] = (f32x4){acc[4][b], acc[5][b], acc[6][b], acc[7][b]};
    }
  }
  __syncthreads();
  {
    const int b = tid >> 7, d = tid & 127;
    float s = 0.f;
#pragma unroll
    for (int ww = 0; ww < 8; ++ww) s += red[ww][b][d];
    qs[b][d] = f2bf(s);
  }
  __syncthreads();
  {
    f32x4 a2 = (f32x4){0.f, 0.f, 0.f, 0.f};
#pragma unroll
    for (int s = 0; s < 4; ++s) {
      u32x4 bq = (u32x4){0u, 0u, 0u, 0u};
      if (ch < 4) bq = *(const u32x4*)&qs[ch][32 * s + 8 * g];
      a2 = __builtin_amdgcn_mfma_f32_16x16x32_bf16(as_frag(uk[s]), as_frag(bq), a2, 0, 0, 0);
    }
    if (ch < 4) {
      u32x2 o; o[0] = pack2(a2[0], a2[1]); o[1] = pack2(a2[2], a2[3]);
      *(u32x2*)((u16*)(ws + WS_QABS) + (size_t)(ch * 32 + h) * 512 + c0 + 16 * w + 4 * g) = o;
    }
  }
}

#define LDS_BYTES 163840
#define SPIN_CAP (1u << 16)
#define RAW_BARRIER() do { asm volatile("s_waitcnt lgkmcnt(0)" ::: "memory"); __builtin_amdgcn_s_barrier(); asm volatile("" ::: "memory"); } while (0)
#define GLDS16(src, dst, aux) __builtin_amdgcn_global_load_lds((const unsigned*)(src), (__attribute__((address_space(3))) unsigned*)(dst), 16, 0, (aux))
#define TR_READ(dst, addr, off) asm volatile("ds_read_b64_tr_b16 %0, %1 offset:%2" : "=v"(dst) : "v"(addr), "i"(off) : "memory")
#define B128_READ(dst, addr) asm volatile("ds_read_b128 %0, %1" : "=v"(dst) : "v"(addr) : "memory")

__device__ __forceinline__ void attention_role(const u16* __restrict__ ckv, unsigned char* __restrict__ ws,
                                               unsigned char* smem, int u) {
  const int tid = threadIdx.x, l = tid & 63, g = l >> 4, ch = l & 15;
  const int w = __builtin_amdgcn_readfirstlane(tid >> 6);
  const int b = u >> 5, chunk = u & 31;
  unsigned char* sC = smem;
  unsigned char* sQ = smem + 131072;
  const u16* qabs = (const u16*)(ws + WS_QABS);
#pragma unroll
  for (int i = 0; i < 4; ++i) {
    const int hd = 4 * w + i;
    const unsigned c64 = (l & ~15u) | ((l & 15u) ^ fsw(hd & 15));
    GLDS16(qabs + (size_t)(b * 32 + hd) * 512 + c64 * 8, sQ + hd * 1024, 0);
  }
  {
    const u16* crow = ckv + (size_t)(b * 4096 + chunk * 128) * 512;
#pragma unroll
    for (int p = 0; p < 4; ++p)
#pragma unroll
      for (int rg = 0; rg < 4; ++rg) {
        const int r = 16 * w + 4 * rg + g;
        const unsigned cc = (unsigned)ch ^ fsw(r & 15);
        GLDS16(crow + (size_t)r * 512 + p * 128 + cc * 8, sC + p * 32768 + (16 * w + 4 * rg) * 256, NT_CKV);
      }
  }
  asm volatile("s_waitcnt vmcnt(16)" ::: "memory");
  RAW_BARRIER();
  u32x4 qf[2][16];
  const unsigned fs = fsw(ch);
#pragma unroll
  for (int t = 0; t < 2; ++t)
#pragma unroll
    for (int s = 0; s < 16; ++s) {
      const unsigned c64 = 4 * s + g;
      qf[t][s] = *(const u32x4*)(sQ + (16 * t + ch) * 1024 + ((c64 & ~15u) | ((c64 & 15u) ^ fs)) * 16);
    }
  RAW_BARRIER();
  f32x4 s0 = (f32x4){0.f, 0.f, 0.f, 0.f}, s1 = (f32x4){0.f, 0.f, 0.f, 0.f};
#pragma unroll
  for (int p = 0; p < 4; ++p) {
    if (p == 0) asm volatile("s_waitcnt vmcnt(12)" ::: "memory");
    else if (p == 1) asm volatile("s_waitcnt vmcnt(8)" ::: "memory");
    else if (p == 2) asm volatile("s_waitcnt vmcnt(4)" ::: "memory");
    else asm volatile("s_waitcnt vmcnt(0)" ::: "memory");
    u32x4 a[4];
#pragma unroll
    for (int ss = 0; ss < 4; ++ss)
      a[ss] = *(const u32x4*)(sC + p * 32768 + (16 * w + ch) * 256 + ((unsigned)(4 * ss + g) ^ fs) * 16);
#pragma unroll
    for (int ss = 0; ss < 4; ++ss) {
      s0 = __builtin_amdgcn_mfma_f32_16x16x32_bf16(as_frag(a[ss]), as_frag(qf[0][4 * p + ss]), s0, 0, 0, 0);
      s1 = __builtin_amdgcn_mfma_f32_16x16x32_bf16(as_frag(a[ss]), as_frag(qf[1][4 * p + ss]), s1, 0, 0, 0);
    }
  }
  unsigned char* sP = sQ;
  float* wmax = (float*)(sQ + 8192);
  float* wsum = (float*)(sQ + 9216);
  {
    float m0 = fmaxf(fmaxf(s0[0], s0[1]), fmaxf(s0[2], s0[3]));
    float m1 = fmaxf(fmaxf(s1[0], s1[1]), fmaxf(s1[2], s1[3]));
    m0 = x32_max(x16_max(m0)); m1 = x32_max(x16_max(m1));
    if (g == 0) wmax[ch * 8 + w] = m0;
    if (g == 1) wmax[(16 + ch) * 8 + w] = m1;
  }
  RAW_BARRIER();
  const float cs = 0.08838834764831845f * 1.4426950408889634f;
  {
    const f32x4 a0 = *(const f32x4*)&wmax[ch * 8], a1 = *(const f32x4*)&wmax[ch * 8 + 4];
    const f32x4 b0 = *(const f32x4*)&wmax[(16 + ch) * 8], b1 = *(const f32x4*)&wmax[(16 + ch) * 8 + 4];
    const float M0 = fmaxf(fmaxf(fmaxf(a0[0], a0[1]), fmaxf(a0[2], a0[3])), fmaxf(fmaxf(a1[0], a1[1]), fmaxf(a1[2], a1[3])));
    const float M1 = fmaxf(fmaxf(fmaxf(b0[0], b0[1]), fmaxf(b0[2], b0[3])), fmaxf(fmaxf(b1[0], b1[1]), fmaxf(b1[2], b1[3])));
    u32x2 p0, p1;
    p0[0] = pack2(exp2f((s0[0] - M0) * cs), exp2f((s0[1] - M0) * cs));
    p0[1] = pack2(exp2f((s0[2] - M0) * cs), exp2f((s0[3] - M0) * cs));
    p1[0] = pack2(exp2f((s1[0] - M1) * cs), exp2f((s1[1] - M1) * cs));
    p1[1] = pack2(exp2f((s1[2] - M1) * cs), exp2f((s1[3] - M1) * cs));
    const unsigned off = (((unsigned)(2 * w + (g >> 1)) ^ fs) * 16) + (g & 1) * 8;
    *(u32x2*)(sP + ch * 256 + off) = p0;
    *(u32x2*)(sP + (16 + ch) * 256 + off) = p1;
    float l0 = (lo_f(p0[0]) + hi_f(p0[0])) + (lo_f(p0[1]) + hi_f(p0[1]));
    float l1 = (lo_f(p1[0]) + hi_f(p1[0])) + (lo_f(p1[1]) + hi_f(p1[1]));
    l0 = x32_sum(x16_sum(l0)); l1 = x32_sum(x16_sum(l1));
    if (g == 0) wsum[ch * 8 + w] = l0;
    if (g == 1) wsum[(16 + ch) * 8 + w] = l1;
  }
  RAW_BARRIER();
  const int hd32 = l & 31, hh = l >> 5;
  f32x16 o0, o1;
#pragma unroll
  for (int i = 0; i < 16; ++i) { o0[i] = 0.f; o1[i] = 0.f; }
  {
    const unsigned blk = (l >> 4) & 1, q = (l & 15) >> 2, p4 = l & 3;
    const unsigned sCbase = (unsigned)(size_t)(sC) + (w >> 1) * 32768;
    const unsigned sPbase = (unsigned)(size_t)(sP) + hd32 * 256;
    const unsigned fh = fsw(hd32 & 15);
    unsigned ta[2][2];
#pragma unroll
    for (int tt = 0; tt < 2; ++tt)
#pragma unroll
      for (int t2 = 0; t2 < 2; ++t2) {
        const unsigned row = 8 * hh + 4 * t2 + q;
        const unsigned cc = 4 * (2 * (w & 1) + tt) + 2 * blk + (p4 >> 1);
        ta[tt][t2] = sCbase + row * 256 + ((cc ^ fsw(row)) * 16) + 8 * (p4 & 1);
      }
    u32x4 pb[8]; u32x2 a[8][2][2];
#define PV_ISSUE(ks) do { B128_READ(pb[ks], sPbase + (((unsigned)(2 * (ks) + hh)) ^ fh) * 16); \
      TR_READ(a[ks][0][0], ta[0][0], (ks) * 4096); TR_READ(a[ks][0][1], ta[0][1], (ks) * 4096); \
      TR_READ(a[ks][1][0], ta[1][0], (ks) * 4096); TR_READ(a[ks][1][1], ta[1][1], (ks) * 4096); } while (0)
#define PV_WAIT(ks, n) asm volatile("s_waitcnt lgkmcnt(" #n ")" : "+v"(pb[ks]), "+v"(a[ks][0][0]), "+v"(a[ks][0][1]), "+v"(a[ks][1][0]), "+v"(a[ks][1][1]) :: "memory")
#define PV_MMA(ks) do { \
      u32x4 a0 = (u32x4){a[ks][0][0][0], a[ks][0][0][1], a[ks][0][1][0], a[ks][0][1][1]}; \
      u32x4 a1 = (u32x4){a[ks][1][0][0], a[ks][1][0][1], a[ks][1][1][0], a[ks][1][1][1]}; \
      o0 = __builtin_amdgcn_mfma_f32_32x32x16_bf16(as_frag(a0), as_frag(pb[ks]), o0, 0, 0, 0); \
      o1 = __builtin_amdgcn_mfma_f32_32x32x16_bf16(as_frag(a1), as_frag(pb[ks]), o1, 0, 0, 0); } while (0)
    PV_ISSUE(0); PV_ISSUE(1); PV_ISSUE(2);
    PV_WAIT(0, 10); PV_MMA(0); __builtin_amdgcn_sched_barrier(0); PV_ISSUE(3);
    PV_WAIT(1, 10); PV_MMA(1); __builtin_amdgcn_sched_barrier(0); PV_ISSUE(4);
    PV_WAIT(2, 10); PV_MMA(2); __builtin_amdgcn_sched_barrier(0); PV_ISSUE(5);
    PV_WAIT(3, 10); PV_MMA(3); __builtin_amdgcn_sched_barrier(0); PV_ISSUE(6);
    PV_WAIT(4, 10); PV_MMA(4); __builtin_amdgcn_sched_barrier(0); PV_ISSUE(7);
    PV_WAIT(5, 10); PV_MMA(5);
    PV_WAIT(6, 5);  PV_MMA(6);
    PV_WAIT(7, 0);  PV_MMA(7);
  }
  RAW_BARRIER();
  {
    unsigned char* st = sC + w * 4096;
#pragma unroll
    for (int tt = 0; tt < 2; ++tt)
#pragma unroll
      for (int qq = 0; qq < 4; ++qq) {
        u32x2 v;
        if (tt == 0) { v[0] = pack2(o0[4 * qq], o0[4 * qq + 1]); v[1] = pack2(o0[4 * qq + 2], o0[4 * qq + 3]); }
        else         { v[0] = pack2(o1[4 * qq], o1[4 * qq + 1]); v[1] = pack2(o1[4 * qq + 2], o1[4 * qq + 3]); }
        *(u32x2*)(st + hd32 * 128 + (((unsigned)(4 * tt + qq) ^ (unsigned)(hd32 & 7)) * 16) + 8 * hh) = v;
      }
    const __amdgpu_buffer_rsrc_t prs = __builtin_amdgcn_make_buffer_rsrc(ws + WS_PART, 0, 128 * 32768, 0x00020000);
#pragma unroll
    for (int n = 0; n < 4; ++n) {
      const int hd = 8 * n + (l >> 3);
      u32x4 v = *(const u32x4*)(st + hd * 128 + (((unsigned)(l & 7) ^ (unsigned)(hd & 7)) * 16));
      v[0] = nz(v[0]); v[1] = nz(v[1]); v[2] = nz(v[2]); v[3] = nz(v[3]);
      __builtin_amdgcn_raw_buffer_store_b128(v, prs, (unsigned)((u * 32 + hd) * 1024 + w * 128 + (l & 7) * 16), 0, 16);
    }
    if (tid < 32) {
      const f32x4 a0 = *(const f32x4*)&wmax[tid * 8], a1 = *(const f32x4*)&wmax[tid * 8 + 4];
      const f32x4 b0 = *(const f32x4*)&wsum[tid * 8], b1 = *(const f32x4*)&wsum[tid * 8 + 4];
      const float m = fmaxf(fmaxf(fmaxf(a0[0], a0[1]), fmaxf(a0[2], a0[3])), fmaxf(fmaxf(a1[0], a1[1]), fmaxf(a1[2], a1[3])));
      const float s = ((b0[0] + b0[1]) + (b0[2] + b0[3])) + ((b1[0] + b1[1]) + (b1[2] + b1[3]));
      const unsigned long long v = (unsigned long long)__builtin_bit_cast(unsigned, m * cs) |
                                   ((unsigned long long)__builtin_bit_cast(unsigned, s) << 32);
      __hip_atomic_store((gu64*)(ws + WS_STATS) + (b * 32 + tid) * 32 + chunk, v, RLX_AGENT);
    }
  }
}

__device__ __forceinline__ unsigned nzf(float f) { const unsigned d = __builtin_bit_cast(unsigned, f); return d ? d : 0x80000000u; }

__device__ __forceinline__ void combine_role(const u16* __restrict__ x, const u16* __restrict__ wuv,
                                             const u16* __restrict__ wo, const u16* __restrict__ wd,
                                             const u16* __restrict__ gw, unsigned char* __restrict__ ws,
                                             u16* __restrict__ out, unsigned char* smem, int v) {
  const int tid = threadIdx.x, l = tid & 63, g = l >> 4, ch = l & 15;
  const int w = __builtin_amdgcn_readfirstlane(tid >> 6);
  const int h = (v & 7) + 8 * ((v >> 3) & 3), b = v >> 5;
  u16* cc = (u16*)(smem);
  float* red2 = (float*)(smem + 2048);
  u16* cx = (u16*)(smem + 6144);
  float* red3 = (float*)(smem + 8192);
  if (v < 56 && w < 4) {
    u32x4 rd[8];
#pragma unroll
    for (int j = 0; j < 8; ++j) rd[j] = ld16<NT_FFN>(wd + (size_t)(32 * v + 8 * g + j) * 512 + 128 * w + 8 * ch);
    u32x4 ha = (u32x4){0u, 0u, 0u, 0u};
    if (ch < 4) ha = *(const u32x4*)((const u16*)(ws + WS_HBUF) + ch * 1792 + 32 * v + 8 * g);
    const __amdgpu_buffer_rsrc_t frs = __builtin_amdgcn_make_buffer_rsrc(ws + WS_FPART, 0, 56 * 8192, 0x00020000);
    f32x4 acc[8];
#pragma unroll
    for (int i = 0; i < 8; ++i) {
      acc[i] = (f32x4){0.f, 0.f, 0.f, 0.f};
      acc[i] = __builtin_amdgcn_mfma_f32_16x16x32_bf16(as_frag(ha), colfrag(rd, i), acc[i], 0, 0, 0);
    }
    if (l < 16) {
#pragma unroll
      for (int bb = 0; bb < 4; ++bb) {
        const unsigned off = (unsigned)(((v * 4 + bb) * 512 + 128 * w + 8 * ch) * 4);
        __builtin_amdgcn_raw_buffer_store_b128((u32x4){nzf(acc[0][bb]), nzf(acc[1][bb]), nzf(acc[2][bb]), nzf(acc[3][bb])}, frs, off, 0, 16);
        __builtin_amdgcn_raw_buffer_store_b128((u32x4){nzf(acc[4][bb]), nzf(acc[5][bb]), nzf(acc[6][bb]), nzf(acc[7][bb])}, frs, off + 16, 0, 16);
      }
    }
  }
  u32x4 r[2][8], ro[2][8];
#pragma unroll
  for (int kb = 0; kb < 2; ++kb)
#pragma unroll
    for (int j = 0; j < 8; ++j)
      r[kb][j] = ld16<NT_WUV>(wuv + (size_t)(64 * w + 32 * kb + 8 * g + j) * 4096 + h * 128 + ch * 8);
#pragma unroll
  for (int cg = 0; cg < 2; ++cg)
#pragma unroll
    for (int j = 0; j < 8; ++j)
      ro[cg][j] = ld16<NT_WO>(wo + (size_t)(h * 128 + 32 * (w >> 1) + 8 * g + j) * 512 + 128 * (2 * (w & 1) + cg) + ch * 8);
  float gate = 0.f, f[4] = {0.f, 0.f, 0.f, 0.f};
  if (v < 64 && w == 7) {
    const int rb = v >> 4, cs0 = (v & 15) * 32;
    const int i8 = l >> 3, c4 = l & 7;
    const u32x4 xv = *(const u32x4*)(x + rb * 512 + 8 * l), gv = *(const u32x4*)(gw + 8 * l);
    float gs = 0.f;
#pragma unroll
    for (int j = 0; j < 4; ++j) { gs += bfr(lo_f(xv[j]) * lo_f(gv[j])); gs += bfr(hi_f(xv[j]) * hi_f(gv[j])); }
    gs += dpp<XOR1>(gs); gs += dpp<XOR2>(gs); gs += dpp<ROR4>(gs); gs += dpp<ROR8>(gs);
    gs = x32_sum(x16_sum(gs));
    gate = bfr(1.f / (1.f + __expf(-bfr(gs))));
    const __amdgpu_buffer_rsrc_t frs = __builtin_amdgcn_make_buffer_rsrc(ws + WS_FPART, 0, 56 * 8192, 0x00020000);
    u32x4 fv[7];
    for (unsigned spins = 0; spins < SPIN_CAP; ++spins) {
      asm volatile("" ::: "memory");
      bool ok = true;
#pragma unroll
      for (int n = 0; n < 7; ++n) {
        fv[n] = __builtin_amdgcn_raw_buffer_load_b128(frs, (unsigned)((((8 * n + i8) * 4 + rb) * 512 + cs0 + 4 * c4) * 4), 0, 16);
        ok = ok && allnz(fv[n]);
      }
      if (__all(ok)) break;
    }
#pragma unroll
    for (int j = 0; j < 4; ++j) {
      f[j] = 0.f;
#pragma unroll
      for (int n = 0; n < 7; ++n) f[j] += __uint_as_float(fv[n][j]);
      f[j] += dpp<ROR8>(f[j]); f[j] = x32_sum(x16_sum(f[j]));
    }
  }
  {
    gu64* sp = (gu64*)(ws + WS_STATS) + (b * 32 + h) * 32 + (l & 31);
    unsigned long long st = 0;
    for (unsigned spins = 0; spins < SPIN_CAP; ++spins) {
      st = __hip_atomic_load(sp, RLX_AGENT);
      if (__all((unsigned)(st >> 32) != 0u)) break;
      __builtin_amdgcn_s_sleep(1);
    }
    const __amdgpu_buffer_rsrc_t prs = __builtin_amdgcn_make_buffer_rsrc(ws + WS_PART, 0, 128 * 32768, 0x00020000);
    u32x4 pv[4];
    for (unsigned spins = 0; spins < SPIN_CAP; ++spins) {
      asm volatile("" ::: "memory");
#pragma unroll
      for (int n = 0; n < 4; ++n)
        pv[n] = __builtin_amdgcn_raw_buffer_load_b128(prs, (unsigned)(((b * 32 + 8 * n + (l >> 3)) * 32 + h) * 1024 + w * 128 + (l & 7) * 16), 0, 16);
      if (__all(allnz(pv[0]) && allnz(pv[1]) && allnz(pv[2]) && allnz(pv[3]))) break;
    }
    const float m = __builtin_bit_cast(float, (unsigned)st), ls = __builtin_bit_cast(float, (unsigned)(st >> 32));
    float M = m;
    M = fmaxf(M, dpp<XOR1>(M)); M = fmaxf(M, dpp<XOR2>(M)); M = fmaxf(M, dpp<ROR4>(M)); M = fmaxf(M, dpp<ROR8>(M));
    M = x16_max(M);
    const float wt = exp2f(m - M);
    float L = wt * ls;
    L += dpp<XOR1>(L); L += dpp<XOR2>(L); L += dpp<ROR4>(L); L += dpp<ROR8>(L);
    L = x16_sum(L);
    const float inv = 1.f / L;
    float a[8];
#pragma unroll
    for (int i = 0; i < 8; ++i) a[i] = 0.f;
#pragma unroll
    for (int n = 0; n < 4; ++n) {
      const float wi = __shfl(wt, 8 * n + (l >> 3));
#pragma unroll
      for (int j = 0; j < 4; ++j) { a[2 * j] += wi * lo_f(pv[n][j]); a[2 * j + 1] += wi * hi_f(pv[n][j]); }
    }
#pragma unroll
    for (int i = 0; i < 8; ++i) { a[i] += dpp<ROR8>(a[i]); a[i] = x32_sum(x16_sum(a[i])) * inv; }
    if (l < 16) {
      u32x4 o;
#pragma unroll
      for (int j = 0; j < 4; ++j) {
        const unsigned hi = pack2(a[2 * j], a[2 * j + 1]);
        o[j] = (l < 8) ? hi : pack2(a[2 * j] - lo_f(hi), a[2 * j + 1] - hi_f(hi));
      }
      *(u32x4*)&cc[(w * 2 + (l >> 3)) * 64 + 8 * (l & 7)] = o;
    }
  }
  {
    f32x4 acc[8];
#pragma unroll
    for (int i = 0; i < 8; ++i) acc[i] = (f32x4){0.f, 0.f, 0.f, 0.f};
#pragma unroll
    for (int kb = 0; kb < 2; ++kb) {
      u32x4 xa = (u32x4){0u, 0u, 0u, 0u};
      if (ch < 2) xa = *(const u32x4*)&cc[(w * 2 + ch) * 64 + 32 * kb + 8 * g];
#pragma unroll
      for (int i = 0; i < 8; ++i) acc[i] = __builtin_amdgcn_mfma_f32_16x16x32_bf16(as_frag(xa), colfrag(r[kb], i), acc[i], 0, 0, 0);
    }
    if (l < 16) {
      *(f32x4*)&red2[w * 128 + 8 * ch]     = (f32x4){acc[0][0] + acc[0][1], acc[1][0] + acc[1][1], acc[2][0] + acc[2][1], acc[3][0] + acc[3][1]};
      *(f32x4*)&red2[w * 128 + 8 * ch + 4] = (f32x4){acc[4][0] + acc[4][1], acc[5][0] + acc[5][1], acc[6][0] + acc[6][1], acc[7][0] + acc[7][1]};
    }
  }
  __syncthreads();
  u32x4 xa = (u32x4){0u, 0u, 0u, 0u};
  {
    float s[8];
#pragma unroll
    for (int j = 0; j < 8; ++j) s[j] = 0.f;
    const int d0 = 32 * (w >> 1) + 8 * g;
#pragma unroll
    for (int ww = 0; ww < 8; ++ww) {
      const f32x4 u0 = *(const f32x4*)&red2[ww * 128 + d0], u1 = *(const f32x4*)&red2[ww * 128 + d0 + 4];
      s[0] += u0[0]; s[1] += u0[1]; s[2] += u0[2]; s[3] += u0[3]; s[4] += u1[0]; s[5] += u1[1]; s[6] += u1[2]; s[7] += u1[3];
    }
    if (ch == 0) xa = (u32x4){pack2(s[0], s[1]), pack2(s[2], s[3]), pack2(s[4], s[5]), pack2(s[6], s[7])};
  }
  {
#pragma unroll
    for (int cg = 0; cg < 2; ++cg) {
      f32x4 acc[8];
#pragma unroll
      for (int i = 0; i < 8; ++i) {
        acc[i] = (f32x4){0.f, 0.f, 0.f, 0.f};
        acc[i] = __builtin_amdgcn_mfma_f32_16x16x32_bf16(as_frag(xa), colfrag(ro[cg], i), acc[i], 0, 0, 0);
      }
      if (l < 16) {
        float* d = &red3[(w >> 1) * 512 + 128 * (2 * (w & 1) + cg) + 8 * ch];
        *(f32x4*)d = (f32x4){acc[0][0], acc[1][0], acc[2][0], acc[3][0]};
        *(f32x4*)(d + 4) = (f32x4){acc[4][0], acc[5][0], acc[6][0], acc[7][0]};
      }
    }
  }
  __syncthreads();
  if (tid < 128) {
    const f32x4 u0 = *(const f32x4*)&red3[4 * tid], u1 = *(const f32x4*)&red3[512 + 4 * tid];
    const f32x4 u2 = *(const f32x4*)&red3[1024 + 4 * tid], u3 = *(const f32x4*)&red3[1536 + 4 * tid];
    const __amdgpu_buffer_rsrc_t ors = __builtin_amdgcn_make_buffer_rsrc(ws + WS_OPART, 0, 128 * 2048, 0x00020000);
    u32x4 o;
#pragma unroll
    for (int j = 0; j < 4; ++j) o[j] = nzf((u0[j] + u1[j]) + (u2[j] + u3[j]));
    __builtin_amdgcn_raw_buffer_store_b128(o, ors, (unsigned)(((b * 32 + h) * 512 + 4 * tid) * 4), 0, 16);
  }
  if (v < 64 && w == 7) {
    const int rb = v >> 4, cs0 = (v & 15) * 32;
    const int i8 = l >> 3, c4 = l & 7;
    const __amdgpu_buffer_rsrc_t ors = __builtin_amdgcn_make_buffer_rsrc(ws + WS_OPART, 0, 128 * 2048, 0x00020000);
    u32x4 av[4];
    for (unsigned spins = 0; spins < SPIN_CAP; ++spins) {
      asm volatile("" ::: "memory");
#pragma unroll
      for (int n = 0; n < 4; ++n)
        av[n] = __builtin_amdgcn_raw_buffer_load_b128(ors, (unsigned)(((rb * 32 + 8 * n + i8) * 512 + cs0 + 4 * c4) * 4), 0, 16);
      if (__all(allnz(av[0]) && allnz(av[1]) && allnz(av[2]) && allnz(av[3]))) break;
    }
    float res[4];
#pragma unroll
    for (int j = 0; j < 4; ++j) {
      float a = (__uint_as_float(av[0][j]) + __uint_as_float(av[1][j])) +
                (__uint_as_float(av[2][j]) + __uint_as_float(av[3][j]));
      a += dpp<ROR8>(a); a = x32_sum(x16_sum(a));
      res[j] = bfr(gate * bfr(a)) + bfr(bfr(1.f - gate) * bfr(f[j]));
    }
    if (l < 8) *(u32x2*)(out + rb * 512 + cs0 + 4 * l) = (u32x2){pack2(res[0], res[1]), pack2(res[2], res[3])};
  }
}

__global__ __launch_bounds__(512, 2) void k2_kernel(const u16* __restrict__ x, const u16* __restrict__ ckv,
                                                    const u16* __restrict__ wuv, const u16* __restrict__ wo,
                                                    const u16* __restrict__ wd, const u16* __restrict__ gw,
                                                    unsigned char* __restrict__ ws, u16* __restrict__ out) {
  extern __shared__ __attribute__((aligned(16))) unsigned char smem[];
  const int bid = blockIdx.x;
  if (bid < 128) attention_role(ckv, ws, smem, bid);
  else combine_role(x, wuv, wo, wd, gw, ws, out, smem, bid - 128);
}

extern "C" void kernel_launch(void* const* d_in, const int* in_sizes, int n_in,
                              void* d_out, int out_size, void* d_ws, size_t ws_size,
                              hipStream_t stream) {
  const u16* x   = (const u16*)d_in[0];
  const u16* ckv = (const u16*)d_in[1];
  const u16* wq  = (const u16*)d_in[2];
  const u16* wuk = (const u16*)d_in[3];
  const u16* wuv = (const u16*)d_in[4];
  const u16* wo  = (const u16*)d_in[5];
  const u16* wg  = (const u16*)d_in[7];
  const u16* wu  = (const u16*)d_in[8];
  const u16* wd  = (const u16*)d_in[9];
  const u16* gw  = (const u16*)d_in[10];
  unsigned char* ws = (unsigned char*)d_ws;
  (void)hipFuncSetAttribute((const void*)k2_kernel, hipFuncAttributeMaxDynamicSharedMemorySize, LDS_BYTES);
  k1_kernel<<<128 + K1_FFN_BLOCKS + K1_ZERO_BLOCKS, 512, 0, stream>>>(x, wq, wuk, wg, wu, ws);
  k2_kernel<<<256, 512, LDS_BYTES, stream>>>(x, ckv, wuv, wo, wd, gw, ws, (u16*)d_out);
}
```
